```python
import torch

def _softmax(x, axis=-1):
    return torch.softmax(x, dim=axis)
def _gelu(x):
    return torch.nn.functional.gelu(x, approximate="tanh")
def _silu(x):
    return torch.nn.functional.silu(x)
def _sigmoid(x):
    return torch.sigmoid(x)
def _layernorm(x, axis=-1, eps=1e-5):
    m = x.mean(dim=axis, keepdim=True)
    v = x.var(dim=axis, keepdim=True, unbiased=False)
    return (x - m) / torch.sqrt(v + eps)
def _scatter_add(tgt, idx, src, axis=0):
    return tgt.clone().index_add_(axis, idx, src)
def _segment_sum(src, seg, n):
    out = torch.zeros((n,) + src.shape[1:], dtype=src.dtype)
    return out.index_add_(0, seg, src)
def _pad(x, pw):
    flat = tuple(v for pair in reversed(pw) for v in pair)
    return torch.nn.functional.pad(x, flat)

kernel_name = 'interleave_layers__ssm_conv_h3__glu_gated_glu__prompt'

def setup_inputs():

    _w = (lambda a: (lambda t: t.to(torch.bfloat16) if t.is_floating_point() else t)(torch.as_tensor(a)))
    def _A_setup():
        B, S, D = 1, 2048, 640
        ks = 4
        return {
            "q": _w((lambda *s: torch.randn(*s, dtype=torch.bfloat16) * 0.05)(B, S, D)),
            "k": _w((lambda *s: torch.randn(*s, dtype=torch.bfloat16) * 0.4)(B, S, D)),
            "v": _w((lambda *s: torch.randn(*s, dtype=torch.bfloat16) * 0.4)(B, S, D)),
            "shift_k": _w((lambda *s: torch.randn(*s, dtype=torch.bfloat16) * 0.4)(D, ks) * 0.5),
            "ssm_k": _w((lambda *s: torch.randn(*s, dtype=torch.bfloat16) * 0.4)(D, S) * (1.0 / S ** 0.5)),
            "ssm_D": _w((lambda *s: torch.randn(*s, dtype=torch.bfloat16) * 0.4)(D)),
        }
    _A_in = _A_setup()

    def _B_setup():
        B, S, D, F = 1, 2048, 640, 1728
        return {
            "x": _w((lambda *s: torch.randn(*s, dtype=torch.bfloat16) * 0.05)(B, S, D)),
            "w_gate": _w((lambda *s: torch.randn(*s, dtype=torch.bfloat16) * 0.05)(D, F)),
            "w_up": _w((lambda *s: torch.randn(*s, dtype=torch.bfloat16) * 0.05)(D, F)),
            "w_down": _w((lambda *s: torch.randn(*s, dtype=torch.bfloat16) * 0.05)(F, D)),
        }
    _B_in = _B_setup()

    return {
        "A_q": _A_in["q"],
        "A_k": _A_in["k"],
        "A_v": _A_in["v"],
        "A_shift_k": _A_in["shift_k"],
        "A_ssm_k": _A_in["ssm_k"],
        "A_ssm_D": _A_in["ssm_D"],
        "B_x": _B_in["x"],
        "B_w_gate": _B_in["w_gate"],
        "B_w_up": _B_in["w_up"],
        "B_w_down": _B_in["w_down"],
    }

def reference(A_q, A_k, A_v, A_shift_k, A_ssm_k, A_ssm_D, B_x, B_w_gate, B_w_up, B_w_down):
    def _A_ref(q, k, v, shift_k, ssm_k, ssm_D):
        B, S, D = q.shape
        ks = shift_k.shape[-1]
        k_pad = _pad(k, ((0, 0), (ks - 1, 0), (0, 0)))
        k_shift = torch.zeros_like(k)
        for t in range(ks):
            k_shift = k_shift + k_pad[:, t:t + S, :] * shift_k[:, ks - 1 - t][None, None, :]
        kv = k_shift * v
        L = 2 * S
        kv_pad = _pad(kv, ((0, 0), (0, S), (0, 0)))
        kf_pad = _pad(ssm_k, ((0, 0), (0, S)))
        kv_f = (lambda x, axes=(-2,-1): torch.fft.rfft2(x.float(), dim=axes))(kv_pad[:, None, :, :], axes=(1, 2))
        kf_f = (lambda x, axes=(-2,-1): torch.fft.rfft2(x.float(), dim=axes))(kf_pad[:, None, :], axes=(1, 2))
        kf_f = (lambda x, *d: x.permute(*d))(kf_f, 1, 2, 0)
        y_f = kv_f * kf_f[None, :, :, :]
        y = (lambda x, s, axes=(-2,-1): torch.fft.irfft2(x, s=s, dim=axes).to(torch.bfloat16))(y_f, (1, L), axes=(1, 2))[:, 0, :S, :]
        y = y + kv * ssm_D[None, None, :]
        return q * y

    def _B_ref(x, w_gate, w_up, w_down):
        g = torch.einsum("bsd,df->bsf", x, w_gate)
        u = torch.einsum("bsd,df->bsf", x, w_up)
        h = _sigmoid(g) * u
        return torch.einsum("bsf,fd->bsd", h, w_down)

    x = A_q
    x = x + _A_ref(_layernorm(x, axis=-1), A_k, A_v, A_shift_k, A_ssm_k, A_ssm_D)
    x = x + _B_ref(_layernorm(x, axis=-1), B_w_gate, B_w_up, B_w_down)
    x = x + _A_ref(_layernorm(x, axis=-1), A_k, A_v, A_shift_k, A_ssm_k, A_ssm_D)
    x = x + _B_ref(_layernorm(x, axis=-1), B_w_gate, B_w_up, B_w_down)
    return x
```

```cpp
#include <hip/hip_runtime.h>
#include <hip/hip_bf16.h>
#include <stdint.h>

typedef unsigned short u16;
typedef unsigned int u32;
typedef __attribute__((ext_vector_type(8))) short bf16x8;
typedef __attribute__((ext_vector_type(4))) float f32x4;
typedef __attribute__((ext_vector_type(16))) float f32x16;
typedef __attribute__((ext_vector_type(4))) u32 u32x4;
typedef __attribute__((ext_vector_type(2))) u32 u32x2;
typedef __attribute__((ext_vector_type(2))) float f32x2;
typedef __attribute__((ext_vector_type(2))) __bf16 bf16x2v;

constexpr int S_ = 2048, D_ = 640, F_ = 1728;
constexpr int NUNIT = 108;
constexpr int KS1 = 40;
constexpr int KS2 = 54;

__device__ __forceinline__ float lo2f(u32 v) { return __uint_as_float(v << 16); }
__device__ __forceinline__ float hi2f(u32 v) { return __uint_as_float(v & 0xffff0000u); }
__device__ __forceinline__ u32 pack2(float a, float b) {
    f32x2 f = {a, b};
    bf16x2v r = __builtin_convertvector(f, bf16x2v);
    return __builtin_bit_cast(u32, r);
}
__device__ __forceinline__ float rbf(float a) {
    __bf16 b = (__bf16)a;
    return __uint_as_float(((u32)__builtin_bit_cast(u16, b)) << 16);
}
__device__ __forceinline__ u16 f2bf(float a) {
    __bf16 b = (__bf16)a;
    return __builtin_bit_cast(u16, b);
}

__device__ __forceinline__ float rowsum16(float x) {
    x += __builtin_bit_cast(float, __builtin_amdgcn_update_dpp(0, __builtin_bit_cast(int, x), 0x128, 0xf, 0xf, false));
    x += __builtin_bit_cast(float, __builtin_amdgcn_update_dpp(0, __builtin_bit_cast(int, x), 0x124, 0xf, 0xf, false));
    x += __builtin_bit_cast(float, __builtin_amdgcn_update_dpp(0, __builtin_bit_cast(int, x), 0x122, 0xf, 0xf, false));
    x += __builtin_bit_cast(float, __builtin_amdgcn_update_dpp(0, __builtin_bit_cast(int, x), 0x121, 0xf, 0xf, false));
    return x;
}

__global__ __launch_bounds__(256) void prep_kernel(const u16* __restrict__ k, const u16* __restrict__ v,
                                                   const u16* __restrict__ sk, u16* __restrict__ kvR) {
    __shared__ __attribute__((aligned(16))) u32 T[64 * 32];
    const int st = blockIdx.x / 10, dt = blockIdx.x % 10;
    const int s0 = st * 64, d0 = dt * 64;
    const int t = threadIdx.x, dc = t & 7, rp = t >> 3;
    const int s = s0 + 2 * rp, d = d0 + 8 * dc;
    u32x4 kr[5];
#pragma unroll
    for (int i = 0; i < 5; ++i) {
        int row = s - 3 + i;
        u32x4 z = {0u, 0u, 0u, 0u};
        kr[i] = (row >= 0) ? *(const u32x4*)(k + (long)row * D_ + d) : z;
    }
    u32x4 v0 = *(const u32x4*)(v + (long)s * D_ + d);
    u32x4 v1 = *(const u32x4*)(v + (long)(s + 1) * D_ + d);
    u32x4 skv[4];
#pragma unroll
    for (int i = 0; i < 4; ++i) skv[i] = *(const u32x4*)(sk + (long)d * 4 + 8 * i);
    const int p = 31 - rp;
#pragma unroll
    for (int e = 0; e < 8; ++e) {
        u32 w01 = skv[e >> 1][(e & 1) * 2 + 0], w23 = skv[e >> 1][(e & 1) * 2 + 1];
        float c0 = lo2f(w01), c1 = hi2f(w01), c2 = lo2f(w23), c3 = hi2f(w23);
        float kk[5];
#pragma unroll
        for (int i = 0; i < 5; ++i) {
            u32 wv = kr[i][e >> 1];
            kk[i] = (e & 1) ? hi2f(wv) : lo2f(wv);
        }
        float va = (e & 1) ? hi2f(v0[e >> 1]) : lo2f(v0[e >> 1]);
        float vb = (e & 1) ? hi2f(v1[e >> 1]) : lo2f(v1[e >> 1]);
        float a = rbf(kk[0] * c3);
        a = rbf(a + rbf(kk[1] * c2));
        a = rbf(a + rbf(kk[2] * c1));
        a = rbf(a + rbf(kk[3] * c0));
        float b = rbf(kk[1] * c3);
        b = rbf(b + rbf(kk[2] * c2));
        b = rbf(b + rbf(kk[3] * c1));
        b = rbf(b + rbf(kk[4] * c0));
        u32 pk = pack2(b * vb, a * va);
        int dl = 8 * dc + e;
        T[dl * 32 + ((((p >> 2) ^ dc)) << 2) + (p & 3)] = pk;
    }
    __syncthreads();
#pragma unroll
    for (int it = 0; it < 2; ++it) {
        int idx = t + 256 * it;
        int dl = idx >> 3, c = idx & 7;
        u32x4 o = *(const u32x4*)&T[dl * 32 + ((c ^ (dl >> 3)) << 2)];
        *(u32x4*)(kvR + (long)(d0 + dl) * S_ + (1984 - s0) + 8 * c) = o;
    }
}

constexpr int HC_STRIDE = 4416;
constexpr int KV_BYTES = 47 * 144;
constexpr int WAVE_LDS = 4 * HC_STRIDE + KV_BYTES;
constexpr int YBUF_OFF = 4 * WAVE_LDS;
constexpr int CONV_LDS = YBUF_OFF + 2048 * 8;
constexpr int NCONV = 160;
constexpr int NREP1 = 160;
constexpr int NREP2 = 108;

__global__ __launch_bounds__(256, 1) void conv_kernel(const u16* __restrict__ kvR, const u16* __restrict__ ssmk,
                                                      const u16* __restrict__ ssmD, u16* __restrict__ y,
                                                      const u16* __restrict__ wg, const u16* __restrict__ wu,
                                                      const u16* __restrict__ wd, u16* __restrict__ W1p,
                                                      u16* __restrict__ W2p) {
    __shared__ __attribute__((aligned(16))) char lds[CONV_LDS];
    const int tid = threadIdx.x, lane = tid & 63, w = tid >> 6;
    const int bid = blockIdx.x;
    if (bid < NCONV) {
        const int ch = bid * 4 + w;
        char* wl = lds + w * WAVE_LDS;
        char* kvl = wl + 4 * HC_STRIDE;
        const u32x4* hsrc = (const u32x4*)(ssmk + (long)ch * S_);
        const u32x4* ksrc = (const u32x4*)(kvR + (long)ch * S_);
        u32x4 kvv[4];
#pragma unroll
        for (int m = 0; m < 4; ++m) kvv[m] = ksrc[lane + 64 * m];
#pragma unroll
        for (int m = 0; m < 5; ++m) {
            int c = lane - 8 + 64 * m;
            if (c < 256) {
                u32x4 z = {0u, 0u, 0u, 0u};
                u32x4 a = (c >= 0) ? hsrc[c] : z;
                u32x4 b = (c + 1 >= 0 && c + 1 < 256) ? hsrc[c + 1] : z;
                u32 ww[8] = {a[0], a[1], a[2], a[3], b[0], b[1], b[2], b[3]};
                u32x4 o0, o1, o2, o3;
#pragma unroll
                for (int i = 0; i < 4; ++i) {
                    o0[i] = __builtin_amdgcn_alignbit(ww[i + 1], ww[i], 16);
                    o1[i] = ww[i + 1];
                    o2[i] = __builtin_amdgcn_alignbit(ww[i + 2], ww[i + 1], 16);
                    o3[i] = ww[i + 2];
                }
                char* dst = wl + (c + 8) * 16;
                *(u32x4*)(dst + 0 * HC_STRIDE) = o0;
                *(u32x4*)(dst + 1 * HC_STRIDE) = o1;
                *(u32x4*)(dst + 2 * HC_STRIDE) = o2;
                *(u32x4*)(dst + 3 * HC_STRIDE) = o3;
            }
        }
#pragma unroll
        for (int m = 0; m < 4; ++m) {
            int c = lane + 64 * m;
            *(u32x4*)(kvl + (c >> 3) * 144 + (c & 7) * 16) = kvv[m];
        }
        {
            u32x4 z = {0u, 0u, 0u, 0u};
#pragma unroll
            for (int m = 0; m < 3; ++m) {
                int c = lane + 64 * m;
                if (c < 135) *(u32x4*)(kvl + 32 * 144 + c * 16) = z;
            }
        }
        __builtin_amdgcn_s_waitcnt(0xC07F);
        __builtin_amdgcn_wave_barrier();

        const int r = lane & 15, kq = lane >> 4;
        u32 aoff0 = (u32)(w * WAVE_LDS + (r & 3) * HC_STRIDE + 2 * ((r & 12) + 8 * kq));
        u32 aoff1 = aoff0 + 8;
        asm volatile("" : "+v"(aoff1));
        const u32 boff = (u32)(w * WAVE_LDS + 4 * HC_STRIDE + 16 * (9 * (31 - r) + kq));

        f32x4 acc[2][4];
#pragma unroll
        for (int n = 0; n < 2; ++n)
#pragma unroll
            for (int m = 0; m < 4; ++m) acc[n][m] = (f32x4){0.f, 0.f, 0.f, 0.f};

        bf16x8 A[132];
#pragma unroll
        for (int i = 0; i < 2; ++i) {
            u32x2 l0 = *(const u32x2*)(lds + aoff0 + 32 * i);
            u32x2 l1 = *(const u32x2*)(lds + aoff1 + 32 * i);
            u32x4 t4 = {l0[0], l0[1], l1[0], l1[1]};
            A[i] = __builtin_bit_cast(bf16x8, t4);
        }
#pragma unroll
        for (int ks = 0; ks < 64; ++ks) {
#pragma unroll
            for (int i = 2 * ks + 2; i < 2 * ks + 4; ++i) {
                u32x2 l0 = *(const u32x2*)(lds + aoff0 + 32 * i);
                u32x2 l1 = *(const u32x2*)(lds + aoff1 + 32 * i);
                u32x4 t4 = {l0[0], l0[1], l1[0], l1[1]};
                A[i] = __builtin_bit_cast(bf16x8, t4);
            }
            const int kso = 16 * (9 * (ks >> 1) + 4 * (ks & 1));
            bf16x8 B1 = *(const bf16x8*)(lds + boff + kso - 16 * 9 * 16);
#pragma unroll
            for (int m = 0; m < 4; ++m)
                acc[1][m] = __builtin_amdgcn_mfma_f32_16x16x32_bf16(A[2 * ks + m], B1, acc[1][m], 0, 0, 0);
            if (ks < 32) {
                bf16x8 B0 = *(const bf16x8*)(lds + boff + kso);
#pragma unroll
                for (int m = 0; m < 4; ++m)
                    acc[0][m] = __builtin_amdgcn_mfma_f32_16x16x32_bf16(A[2 * ks + m], B0, acc[0][m], 0, 0, 0);
            }
        }
        const float Dv = lo2f((u32)ssmD[ch]);
        u16* ybuf = (u16*)(lds + YBUF_OFF);
#pragma unroll
        for (int n = 0; n < 2; ++n)
#pragma unroll
            for (int m = 0; m < 4; ++m) {
                int a = 16 * n + r;
                int ib = 60 - 16 * m - 4 * kq;
                u32x2 kk = *(const u32x2*)(kvl + (31 - a) * 144 + ib * 2);
                float kvf[4] = {hi2f(kk[1]), lo2f(kk[1]), hi2f(kk[0]), lo2f(kk[0])};
                int sb = 64 * a + 16 * m + 4 * kq;
#pragma unroll
                for (int j = 0; j < 4; ++j) {
                    float yc = rbf(acc[n][m][j]);
                    float tt = rbf(kvf[j] * Dv);
                    ybuf[(sb + j) * 4 + w] = f2bf(yc + tt);
                }
            }
        __syncthreads();
#pragma unroll
        for (int it = 0; it < 8; ++it) {
            int s = tid + 256 * it;
            u32x2 o = *(const u32x2*)(ybuf + s * 4);
            *(u32x2*)(y + (long)s * D_ + bid * 4) = o;
        }
    } else if (bid < NCONV + NREP1) {
        const int b = bid - NCONV;
        const int ks = b >> 2, ug = b & 3;
        u16* T = (u16*)lds;
        constexpr int TS = 440;
        for (int i = tid; i < 1728; i += 256) {
            int mat = i / 864, rem = i % 864, kr = rem / 54, cc = rem % 54;
            const u16* src = (mat ? wu : wg) + (long)(16 * ks + kr) * F_ + 432 * ug + 8 * cc;
            *(u32x4*)(T + (mat * 16 + kr) * TS + 8 * cc) = *(const u32x4*)src;
        }
        __syncthreads();
        for (int i = tid; i < 1728; i += 256) {
            int uu = i >> 6, l = i & 63;
            int ii = l & 31, hh = l >> 5;
            int mat = ii >> 4, col = 16 * uu + (ii & 15);
            const u16* tp = T + (mat * 16 + 8 * hh) * TS + col;
            u32x4 o;
#pragma unroll
            for (int j = 0; j < 4; ++j) o[j] = (u32)tp[(2 * j) * TS] | ((u32)tp[(2 * j + 1) * TS] << 16);
            *(u32x4*)(W1p + ((long)((27 * ug + uu) * KS1 + ks) * 64 + l) * 8) = o;
        }
    } else {
        const int b = bid - NCONV - NREP1;
        const int s = b >> 1, dh = b & 1;
        u16* T = (u16*)lds;
        constexpr int TS = 328;
        for (int i = tid; i < 1280; i += 256) {
            int fr = i / 40, cc = i % 40;
            *(u32x4*)(T + fr * TS + 8 * cc) = *(const u32x4*)(wd + (long)(32 * s + fr) * D_ + 320 * dh + 8 * cc);
        }
        __syncthreads();
        for (int i = tid; i < 1280; i += 256) {
            int ntl = i >> 6, l = i & 63;
            int idx = l & 15, kq = l >> 4;
            int fb = 16 * (kq >> 1) + 4 * (kq & 1);
            const u16* tp = T + 16 * ntl + idx;
            u32x4 o;
            o[0] = (u32)tp[(fb + 0) * TS] | ((u32)tp[(fb + 1) * TS] << 16);
            o[1] = (u32)tp[(fb + 2) * TS] | ((u32)tp[(fb + 3) * TS] << 16);
            o[2] = (u32)tp[(fb + 8) * TS] | ((u32)tp[(fb + 9) * TS] << 16);
            o[3] = (u32)tp[(fb + 10) * TS] | ((u32)tp[(fb + 11) * TS] << 16);
            *(u32x4*)(W2p + ((long)((20 * dh + ntl) * KS2 + s) * 64 + l) * 8) = o;
        }
    }
}

constexpr int XROW = 1296;
constexpr int PD1 = 8;

__device__ __forceinline__ void ln_stats(const u32x4 (&xv)[5], float& mb, float& rinv) {
    float sum = 0.f;
#pragma unroll
    for (int m = 0; m < 5; ++m)
#pragma unroll
        for (int j = 0; j < 4; ++j) sum += lo2f(xv[m][j]) + hi2f(xv[m][j]);
    sum = rowsum16(sum);
    float mean = sum * (1.0f / 640.0f);
    float sq = 0.f;
#pragma unroll
    for (int m = 0; m < 5; ++m)
#pragma unroll
        for (int j = 0; j < 4; ++j) {
            float a = lo2f(xv[m][j]) - mean, b = hi2f(xv[m][j]) - mean;
            sq += a * a + b * b;
        }
    sq = rowsum16(sq);
    float var = sq * (1.0f / 640.0f);
    mb = rbf(mean);
    float den = rbf(__builtin_sqrtf(rbf(rbf(var) + 1e-5f)));
    rinv = 1.0f / den;
}

__global__ __launch_bounds__(512, 2) void gemm1_kernel(const u16* __restrict__ xin, const u16* __restrict__ y,
                                                       const u16* __restrict__ W1p, u16* __restrict__ xr,
                                                       u16* __restrict__ hp) {
    __shared__ __attribute__((aligned(16))) char lds[64 * XROW];
    const int tid = threadIdx.x, lane = tid & 63, w = tid >> 6;
    const int xcd = blockIdx.x & 7, jj = blockIdx.x >> 3;
    const int rt = 8 * (xcd >> 1) + (jj & 7), ct = 4 * (xcd & 1) + (jj >> 3);
    const int nunits = (ct == 7) ? 10 : 14;
    const bool active = (2 * w < nunits);
    const int u0 = ct * 14 + 2 * w;

    const int rq = lane >> 4, c = lane & 15;
    u32x4 xv[2][5], yv[2][5];
#pragma unroll
    for (int p = 0; p < 2; ++p) {
        long row = 64 * rt + 8 * w + 4 * p + rq;
#pragma unroll
        for (int m = 0; m < 5; ++m) {
            xv[p][m] = *(const u32x4*)(xin + row * D_ + 8 * (c + 16 * m));
            yv[p][m] = *(const u32x4*)(y + row * D_ + 8 * (c + 16 * m));
        }
    }
    const bf16x8* wp0 = (const bf16x8*)W1p + (long)(active ? u0 : 0) * KS1 * 64 + lane;
    const bf16x8* wp1 = wp0 + KS1 * 64;
    bf16x8 wa[PD1], wb[PD1];
    if (active) {
#pragma unroll
        for (int i = 0; i < PD1; ++i) { wa[i] = wp0[i * 64]; wb[i] = wp1[i * 64]; }
    }
#pragma unroll
    for (int p = 0; p < 2; ++p) {
        const int lrow = 8 * w + 4 * p + rq;
        float mb, rinv;
        ln_stats(xv[p], mb, rinv);
        u32x4 x1[5];
#pragma unroll
        for (int m = 0; m < 5; ++m)
#pragma unroll
            for (int j = 0; j < 4; ++j) {
                float a = lo2f(xv[p][m][j]), b = hi2f(xv[p][m][j]);
                float qa = rbf(rbf(a - mb) * rinv), qb = rbf(rbf(b - mb) * rinv);
                float ga = rbf(qa * lo2f(yv[p][m][j])), gb = rbf(qb * hi2f(yv[p][m][j]));
                x1[m][j] = pack2(a + ga, b + gb);
            }
        if (ct == 0) {
            long row = 64 * rt + lrow;
#pragma unroll
            for (int m = 0; m < 5; ++m) *(u32x4*)(xr + row * D_ + 8 * (c + 16 * m)) = x1[m];
        }
        float mb2, rinv2;
        ln_stats(x1, mb2, rinv2);
#pragma unroll
        for (int m = 0; m < 5; ++m) {
            u32x4 o;
#pragma unroll
            for (int j = 0; j < 4; ++j) {
                float a = lo2f(x1[m][j]), b = hi2f(x1[m][j]);
                o[j] = pack2(rbf(a - mb2) * rinv2, rbf(b - mb2) * rinv2);
            }
            *(u32x4*)(lds + lrow * XROW + 16 * (c + 16 * m)) = o;
        }
    }
    __syncthreads();
    if (!active) return;

    const int tk = lane & 31, hh = lane >> 5;
    const char* xb0 = lds + tk * XROW + 16 * hh;
    const char* xb1 = xb0 + 32 * XROW;
    f32x16 acc[2][2];
#pragma unroll
    for (int a = 0; a < 2; ++a)
#pragma unroll
        for (int b = 0; b < 2; ++b)
#pragma unroll
            for (int i = 0; i < 16; ++i) acc[a][b][i] = 0.f;
#pragma unroll
    for (int ks = 0; ks < KS1; ++ks) {
        bf16x8 x0 = *(const bf16x8*)(xb0 + 32 * ks);
        bf16x8 x1 = *(const bf16x8*)(xb1 + 32 * ks);
        bf16x8 a = wa[ks % PD1], b = wb[ks % PD1];
        acc[0][0] = __builtin_amdgcn_mfma_f32_32x32x16_bf16(a, x0, acc[0][0], 0, 0, 0);
        acc[0][1] = __builtin_amdgcn_mfma_f32_32x32x16_bf16(a, x1, acc[0][1], 0, 0, 0);
        acc[1][0] = __builtin_amdgcn_mfma_f32_32x32x16_bf16(b, x0, acc[1][0], 0, 0, 0);
        acc[1][1] = __builtin_amdgcn_mfma_f32_32x32x16_bf16(b, x1, acc[1][1], 0, 0, 0);
        if (ks + PD1 < KS1) { wa[ks % PD1] = wp0[(ks + PD1) * 64]; wb[ks % PD1] = wp1[(ks + PD1) * 64]; }
    }
#pragma unroll
    for (int uu = 0; uu < 2; ++uu)
#pragma unroll
        for (int tb = 0; tb < 2; ++tb) {
            float hv[8];
#pragma unroll
            for (int i = 0; i < 8; ++i) {
                float g = rbf(acc[uu][tb][i]);
                float u = rbf(acc[uu][tb][i + 8]);
                float sg = rbf(1.0f / (1.0f + __expf(-g)));
                hv[i] = sg * u;
            }
            u32x4 o = {pack2(hv[0], hv[1]), pack2(hv[2], hv[3]), pack2(hv[4], hv[5]), pack2(hv[6], hv[7])};
            int token = 64 * rt + 32 * tb + tk;
            int u = u0 + uu;
            long idx = ((long)((token >> 4) * KS2 + (u >> 1)) * 64 + (token & 15) + 16 * (2 * (u & 1) + hh));
            *(u32x4*)(hp + idx * 8) = o;
        }
}

constexpr int PROW = 336;
constexpr int PTILE = 64 * PROW;
constexpr int PD2 = 3;

__global__ __launch_bounds__(512, 2) void gemm2_kernel(const u16* __restrict__ hp, const u16* __restrict__ W2p,
                                                       const u16* __restrict__ xr, u16* __restrict__ xout) {
    __shared__ __attribute__((aligned(16))) char lds[4 * PTILE];
    const int tid = threadIdx.x, lane = tid & 63, w = tid >> 6;
    const int xcd = blockIdx.x & 7, jj = blockIdx.x >> 3;
    const int rt = 8 * (xcd >> 1) + (jj & 7), ct = 4 * (xcd & 1) + (jj >> 3);

    const bf16x8* hb = (const bf16x8*)hp + (long)(4 * rt) * KS2 * 64 + lane;
    const bf16x8* wbp = (const bf16x8*)W2p + (long)(5 * ct) * KS2 * 64 + lane;
    const int nsteps = (w < 6) ? 7 : 6;

    bf16x8 hf[PD2][4], wf[PD2][5];
#pragma unroll
    for (int i = 0; i < PD2; ++i) {
        int s = w + 8 * i;
#pragma unroll
        for (int m = 0; m < 4; ++m) hf[i][m] = hb[(long)(m * KS2 + s) * 64];
#pragma unroll
        for (int n = 0; n < 5; ++n) wf[i][n] = wbp[(long)(n * KS2 + s) * 64];
    }
    u32x4 res0, res1;
    {
        int t0 = tid;
        res0 = *(const u32x4*)(xr + (long)(64 * rt + t0 / 10) * D_ + 80 * ct + 8 * (t0 % 10));
        int t1 = tid + 512;
        if (t1 < 640) res1 = *(const u32x4*)(xr + (long)(64 * rt + t1 / 10) * D_ + 80 * ct + 8 * (t1 % 10));
    }
    f32x4 acc[5][4];
#pragma unroll
    for (int n = 0; n < 5; ++n)
#pragma unroll
        for (int m = 0; m < 4; ++m) acc[n][m] = (f32x4){0.f, 0.f, 0.f, 0.f};
#pragma unroll
    for (int i = 0; i < 7; ++i) {
        if (i < nsteps) {
#pragma unroll
            for (int n = 0; n < 5; ++n)
#pragma unroll
                for (int m = 0; m < 4; ++m)
                    acc[n][m] = __builtin_amdgcn_mfma_f32_16x16x32_bf16(wf[i % PD2][n], hf[i % PD2][m], acc[n][m], 0, 0, 0);
            if (i + PD2 < nsteps) {
                int s = w + 8 * (i + PD2);
#pragma unroll
                for (int m = 0; m < 4; ++m) hf[i % PD2][m] = hb[(long)(m * KS2 + s) * 64];
#pragma unroll
                for (int n = 0; n < 5; ++n) wf[i % PD2][n] = wbp[(long)(n * KS2 + s) * 64];
            }
        }
    }
    const int tl = lane & 15, q = lane >> 4;
    char* slot = lds + (w & 3) * PTILE + tl * PROW + q * 16;
    if (w >= 4) {
#pragma unroll
        for (int n = 0; n < 5; ++n)
#pragma unroll
            for (int m = 0; m < 4; ++m) *(f32x4*)(slot + m * 16 * PROW + n * 64) = acc[n][m];
    }
    __syncthreads();
    if (w < 4) {
#pragma unroll
        for (int n = 0; n < 5; ++n)
#pragma unroll
            for (int m = 0; m < 4; ++m) {
                f32x4 o = *(const f32x4*)(slot + m * 16 * PROW + n * 64);
                *(f32x4*)(slot + m * 16 * PROW + n * 64) = o + acc[n][m];
            }
    }
    __syncthreads();
#pragma unroll
    for (int it = 0; it < 2; ++it) {
        int t = tid + 512 * it;
        if (t < 640) {
            int tok = t / 10, cc = t % 10;
            const char* p = lds + tok * PROW + cc * 32;
            f32x4 s0 = *(const f32x4*)(p), s1 = *(const f32x4*)(p + 16);
#pragma unroll
            for (int k = 1; k < 4; ++k) {
                s0 += *(const f32x4*)(p + k * PTILE);
                s1 += *(const f32x4*)(p + k * PTILE + 16);
            }
            u32x4 rs = it ? res1 : res0;
            u32x4 o;
            o[0] = pack2(lo2f(rs[0]) + rbf(s0[0]), hi2f(rs[0]) + rbf(s0[1]));
            o[1] = pack2(lo2f(rs[1]) + rbf(s0[2]), hi2f(rs[1]) + rbf(s0[3]));
            o[2] = pack2(lo2f(rs[2]) + rbf(s1[0]), hi2f(rs[2]) + rbf(s1[1]));
            o[3] = pack2(lo2f(rs[3]) + rbf(s1[2]), hi2f(rs[3]) + rbf(s1[3]));
            *(u32x4*)(xout + (long)(64 * rt + tok) * D_ + 80 * ct + 8 * cc) = o;
        }
    }
}

extern "C" void kernel_launch(void* const* d_in, const int* in_sizes, int n_in,
                              void* d_out, int out_size, void* d_ws, size_t ws_size,
                              hipStream_t stream) {
    const u16* q = (const u16*)d_in[0];
    const u16* k = (const u16*)d_in[1];
    const u16* v = (const u16*)d_in[2];
    const u16* sk = (const u16*)d_in[3];
    const u16* ssmk = (const u16*)d_in[4];
    const u16* ssmD = (const u16*)d_in[5];
    const u16* wg = (const u16*)d_in[7];
    const u16* wu = (const u16*)d_in[8];
    const u16* wd = (const u16*)d_in[9];
    u16* out = (u16*)d_out;

    char* ws = (char*)d_ws;
    constexpr size_t SZ_X = (size_t)S_ * D_ * 2;
    u16* kvR = (u16*)(ws);
    u16* y = (u16*)(ws + SZ_X);
    u16* xr = (u16*)(ws + 2 * SZ_X);
    u16* x2 = (u16*)(ws + 3 * SZ_X);
    u16* W1p = (u16*)(ws + 4 * SZ_X);
    u16* W2p = (u16*)(ws + 4 * SZ_X + (size_t)NUNIT * KS1 * 1024);
    u16* hp = (u16*)(ws + 4 * SZ_X + (size_t)NUNIT * KS1 * 1024 + (size_t)40 * KS2 * 1024);

    prep_kernel<<<320, 256, 0, stream>>>(k, v, sk, kvR);
    conv_kernel<<<NCONV + NREP1 + NREP2, 256, 0, stream>>>(kvR, ssmk, ssmD, y, wg, wu, wd, W1p, W2p);
    gemm1_kernel<<<256, 512, 0, stream>>>(q, y, W1p, xr, hp);
    gemm2_kernel<<<256, 512, 0, stream>>>(hp, W2p, xr, x2);
    gemm1_kernel<<<256, 512, 0, stream>>>(x2, y, W1p, xr, hp);
    gemm2_kernel<<<256, 512, 0, stream>>>(hp, W2p, xr, out);
}
```

```cpp
#include <hip/hip_runtime.h>
#include <hip/hip_bf16.h>
#include <stdint.h>

typedef unsigned short u16;
typedef unsigned int u32;
typedef __attribute__((ext_vector_type(8))) short bf16x8;
typedef __attribute__((ext_vector_type(4))) float f32x4;
typedef __attribute__((ext_vector_type(16))) float f32x16;
typedef __attribute__((ext_vector_type(4))) u32 u32x4;
typedef __attribute__((ext_vector_type(2))) u32 u32x2;
typedef __attribute__((ext_vector_type(2))) float f32x2;
typedef __attribute__((ext_vector_type(2))) __bf16 bf16x2v;

constexpr int S_ = 2048, D_ = 640, F_ = 1728;
constexpr int NUNIT = 108;
constexpr int KS1 = 40;
constexpr int KS2 = 54;

__device__ __forceinline__ float lo2f(u32 v) { return __uint_as_float(v << 16); }
__device__ __forceinline__ float hi2f(u32 v) { return __uint_as_float(v & 0xffff0000u); }
__device__ __forceinline__ u32 pack2(float a, float b) {
    f32x2 f = {a, b};
    bf16x2v r = __builtin_convertvector(f, bf16x2v);
    return __builtin_bit_cast(u32, r);
}
__device__ __forceinline__ float rbf(float a) {
    __bf16 b = (__bf16)a;
    return __uint_as_float(((u32)__builtin_bit_cast(u16, b)) << 16);
}
__device__ __forceinline__ u16 f2bf(float a) {
    __bf16 b = (__bf16)a;
    return __builtin_bit_cast(u16, b);
}

__device__ __forceinline__ float rowsum16(float x) {
    x += __builtin_bit_cast(float, __builtin_amdgcn_update_dpp(0, __builtin_bit_cast(int, x), 0x128, 0xf, 0xf, false));
    x += __builtin_bit_cast(float, __builtin_amdgcn_update_dpp(0, __builtin_bit_cast(int, x), 0x124, 0xf, 0xf, false));
    x += __builtin_bit_cast(float, __builtin_amdgcn_update_dpp(0, __builtin_bit_cast(int, x), 0x122, 0xf, 0xf, false));
    x += __builtin_bit_cast(float, __builtin_amdgcn_update_dpp(0, __builtin_bit_cast(int, x), 0x121, 0xf, 0xf, false));
    return x;
}

__global__ __launch_bounds__(256) void prep_kernel(const u16* __restrict__ k, const u16* __restrict__ v,
                                                   const u16* __restrict__ sk, u16* __restrict__ kvR) {
    __shared__ __attribute__((aligned(16))) u32 T[64 * 32];
    const int st = blockIdx.x / 10, dt = blockIdx.x % 10;
    const int s0 = st * 64, d0 = dt * 64;
    const int t = threadIdx.x, dc = t & 7, rp = t >> 3;
    const int s = s0 + 2 * rp, d = d0 + 8 * dc;
    u32x4 kr[5];
#pragma unroll
    for (int i = 0; i < 5; ++i) {
        int row = s - 3 + i;
        u32x4 z = {0u, 0u, 0u, 0u};
        kr[i] = (row >= 0) ? *(const u32x4*)(k + (long)row * D_ + d) : z;
    }
    u32x4 v0 = *(const u32x4*)(v + (long)s * D_ + d);
    u32x4 v1 = *(const u32x4*)(v + (long)(s + 1) * D_ + d);
    u32x4 skv[4];
#pragma unroll
    for (int i = 0; i < 4; ++i) skv[i] = *(const u32x4*)(sk + (long)d * 4 + 8 * i);
    const int p = 31 - rp;
#pragma unroll
    for (int e = 0; e < 8; ++e) {
        u32 w01 = skv[e >> 1][(e & 1) * 2 + 0], w23 = skv[e >> 1][(e & 1) * 2 + 1];
        float c0 = lo2f(w01), c1 = hi2f(w01), c2 = lo2f(w23), c3 = hi2f(w23);
        float kk[5];
#pragma unroll
        for (int i = 0; i < 5; ++i) {
            u32 wv = kr[i][e >> 1];
            kk[i] = (e & 1) ? hi2f(wv) : lo2f(wv);
        }
        float va = (e & 1) ? hi2f(v0[e >> 1]) : lo2f(v0[e >> 1]);
        float vb = (e & 1) ? hi2f(v1[e >> 1]) : lo2f(v1[e >> 1]);
        float a = rbf(kk[0] * c3);
        a = rbf(a + rbf(kk[1] * c2));
        a = rbf(a + rbf(kk[2] * c1));
        a = rbf(a + rbf(kk[3] * c0));
        float b = rbf(kk[1] * c3);
        b = rbf(b + rbf(kk[2] * c2));
        b = rbf(b + rbf(kk[3] * c1));
        b = rbf(b + rbf(kk[4] * c0));
        u32 pk = pack2(b * vb, a * va);
        int dl = 8 * dc + e;
        T[dl * 32 + ((((p >> 2) ^ dc)) << 2) + (p & 3)] = pk;
    }
    __syncthreads();
#pragma unroll
    for (int it = 0; it < 2; ++it) {
        int idx = t + 256 * it;
        int dl = idx >> 3, c = idx & 7;
        u32x4 o = *(const u32x4*)&T[dl * 32 + ((c ^ (dl >> 3)) << 2)];
        *(u32x4*)(kvR + (long)(d0 + dl) * S_ + (1984 - s0) + 8 * c) = o;
    }
}

constexpr int HC_STRIDE = 4416;
constexpr int KV_BYTES = 47 * 144;
constexpr int WAVE_LDS = 4 * HC_STRIDE + KV_BYTES;
constexpr int YBUF_OFF = 4 * WAVE_LDS;
constexpr int CONV_LDS = YBUF_OFF + 2048 * 8;
constexpr int NCONV = 160;
constexpr int NREP1 = 160;
constexpr int NREP2 = 108;

__global__ __launch_bounds__(256, 1) void conv_kernel(const u16* __restrict__ kvR, const u16* __restrict__ ssmk,
                                                      const u16* __restrict__ ssmD, u16* __restrict__ y,
                                                      const u16* __restrict__ wg, const u16* __restrict__ wu,
                                                      const u16* __restrict__ wd, u16* __restrict__ W1p,
                                                      u16* __restrict__ W2p) {
    __shared__ __attribute__((aligned(16))) char lds[CONV_LDS];
    const int tid = threadIdx.x, lane = tid & 63, w = tid >> 6;
    const int bid = blockIdx.x;
    if (bid < NCONV) {
        const int ch = bid * 4 + w;
        char* wl = lds + w * WAVE_LDS;
        char* kvl = wl + 4 * HC_STRIDE;
        const u32x4* hsrc = (const u32x4*)(ssmk + (long)ch * S_);
        const u32x4* ksrc = (const u32x4*)(kvR + (long)ch * S_);
        u32x4 kvv[4];
#pragma unroll
        for (int m = 0; m < 4; ++m) kvv[m] = ksrc[lane + 64 * m];
#pragma unroll
        for (int m = 0; m < 5; ++m) {
            int c = lane - 8 + 64 * m;
            if (c < 256) {
                u32x4 z = {0u, 0u, 0u, 0u};
                u32x4 a = (c >= 0) ? hsrc[c] : z;
                u32x4 b = (c + 1 >= 0 && c + 1 < 256) ? hsrc[c + 1] : z;
                u32 ww[8] = {a[0], a[1], a[2], a[3], b[0], b[1], b[2], b[3]};
                u32x4 o0, o1, o2, o3;
#pragma unroll
                for (int i = 0; i < 4; ++i) {
                    o0[i] = __builtin_amdgcn_alignbit(ww[i + 1], ww[i], 16);
                    o1[i] = ww[i + 1];
                    o2[i] = __builtin_amdgcn_alignbit(ww[i + 2], ww[i + 1], 16);
                    o3[i] = ww[i + 2];
                }
                char* dst = wl + (c + 8) * 16;
                *(u32x4*)(dst + 0 * HC_STRIDE) = o0;
                *(u32x4*)(dst + 1 * HC_STRIDE) = o1;
                *(u32x4*)(dst + 2 * HC_STRIDE) = o2;
                *(u32x4*)(dst + 3 * HC_STRIDE) = o3;
            }
        }
#pragma unroll
        for (int m = 0; m < 4; ++m) {
            int c = lane + 64 * m;
            *(u32x4*)(kvl + (c >> 3) * 144 + (c & 7) * 16) = kvv[m];
        }
        {
            u32x4 z = {0u, 0u, 0u, 0u};
#pragma unroll
            for (int m = 0; m < 3; ++m) {
                int c = lane + 64 * m;
                if (c < 135) *(u32x4*)(kvl + 32 * 144 + c * 16) = z;
            }
        }
        __builtin_amdgcn_s_waitcnt(0xC07F);
        __builtin_amdgcn_wave_barrier();

        const int r = lane & 15, kq = lane >> 4;
        u32 aoffA = (u32)(w * WAVE_LDS + (r & 3) * HC_STRIDE + 2 * ((r & 12) + 8 * kq));
        u32 aoffB = aoffA + 8, aoffC = aoffA + 32, aoffD = aoffA + 40;
        asm volatile("" : "+v"(aoffB));
        asm volatile("" : "+v"(aoffC));
        asm volatile("" : "+v"(aoffD));
        const u32 boff = (u32)(w * WAVE_LDS + 4 * HC_STRIDE + 16 * (9 * (31 - r) + kq));

        f32x4 acc[2][4];
#pragma unroll
        for (int n = 0; n < 2; ++n)
#pragma unroll
            for (int m = 0; m < 4; ++m) acc[n][m] = (f32x4){0.f, 0.f, 0.f, 0.f};

        bf16x8 A[136], B0[32], B1[64];
#define LOADA(i) do { \
            u32x2 l0 = *(const u32x2*)(lds + (((i) & 1) ? aoffC : aoffA) + 64 * ((i) >> 1)); \
            u32x2 l1 = *(const u32x2*)(lds + (((i) & 1) ? aoffD : aoffB) + 64 * ((i) >> 1)); \
            u32x4 t4 = {l0[0], l0[1], l1[0], l1[1]}; \
            A[i] = __builtin_bit_cast(bf16x8, t4); } while (0)
#define KSO(ks) (16 * (9 * ((ks) >> 1) + 4 * ((ks) & 1)))
#define LOADB(ks) do { \
            B1[ks] = *(const bf16x8*)(lds + boff + KSO(ks) - 16 * 9 * 16); \
            if ((ks) < 32) B0[(ks) < 32 ? (ks) : 0] = *(const bf16x8*)(lds + boff + KSO(ks)); } while (0)
#pragma unroll
        for (int i = 0; i < 6; ++i) LOADA(i);
        LOADB(0);
        LOADB(1);
        __builtin_amdgcn_sched_barrier(0);
#pragma unroll
        for (int ks = 0; ks < 64; ++ks) {
            if (ks + 2 < 64) {
                LOADA(2 * ks + 6);
                LOADA(2 * ks + 7);
                LOADB(ks + 2);
            }
            __builtin_amdgcn_sched_barrier(0);
#pragma unroll
            for (int m = 0; m < 4; ++m)
                acc[1][m] = __builtin_amdgcn_mfma_f32_16x16x32_bf16(A[2 * ks + m], B1[ks], acc[1][m], 0, 0, 0);
            if (ks < 32) {
#pragma unroll
                for (int m = 0; m < 4; ++m)
                    acc[0][m] = __builtin_amdgcn_mfma_f32_16x16x32_bf16(A[2 * ks + m], B0[ks < 32 ? ks : 0], acc[0][m], 0, 0, 0);
            }
            __builtin_amdgcn_sched_barrier(0);
        }
#undef LOADA
#undef LOADB
#undef KSO
        const float Dv = lo2f((u32)ssmD[ch]);
        u16* ybuf = (u16*)(lds + YBUF_OFF);
#pragma unroll
        for (int n = 0; n < 2; ++n)
#pragma unroll
            for (int m = 0; m < 4; ++m) {
                int a = 16 * n + r;
                int ib = 60 - 16 * m - 4 * kq;
                u32x2 kk = *(const u32x2*)(kvl + (31 - a) * 144 + ib * 2);
                float kvf[4] = {hi2f(kk[1]), lo2f(kk[1]), hi2f(kk[0]), lo2f(kk[0])};
                int sb = 64 * a + 16 * m + 4 * kq;
#pragma unroll
                for (int j = 0; j < 4; ++j) {
                    float yc = rbf(acc[n][m][j]);
                    float tt = rbf(kvf[j] * Dv);
                    ybuf[(sb + j) * 4 + w] = f2bf(yc + tt);
                }
            }
        __syncthreads();
#pragma unroll
        for (int it = 0; it < 8; ++it) {
            int s = tid + 256 * it;
            u32x2 o = *(const u32x2*)(ybuf + s * 4);
            *(u32x2*)(y + (long)s * D_ + bid * 4) = o;
        }
    } else if (bid < NCONV + NREP1) {
        const int b = bid - NCONV;
        const int ks = b >> 2, ug = b & 3;
        u16* T = (u16*)lds;
        constexpr int TS = 440;
        for (int i = tid; i < 1728; i += 256) {
            int mat = i / 864, rem = i % 864, kr = rem / 54, cc = rem % 54;
            const u16* src = (mat ? wu : wg) + (long)(16 * ks + kr) * F_ + 432 * ug + 8 * cc;
            *(u32x4*)(T + (mat * 16 + kr) * TS + 8 * cc) = *(const u32x4*)src;
        }
        __syncthreads();
        for (int i = tid; i < 1728; i += 256) {
            int uu = i >> 6, l = i & 63;
            int ii = l & 31, hh = l >> 5;
            int mat = ii >> 4, col = 16 * uu + (ii & 15);
            const u16* tp = T + (mat * 16 + 8 * hh) * TS + col;
            u32x4 o;
#pragma unroll
            for (int j = 0; j < 4; ++j) o[j] = (u32)tp[(2 * j) * TS] | ((u32)tp[(2 * j + 1) * TS] << 16);
            *(u32x4*)(W1p + ((long)((27 * ug + uu) * KS1 + ks) * 64 + l) * 8) = o;
        }
    } else {
        const int b = bid - NCONV - NREP1;
        const int s = b >> 1, dh = b & 1;
        u16* T = (u16*)lds;
        constexpr int TS = 328;
        for (int i = tid; i < 1280; i += 256) {
            int fr = i / 40, cc = i % 40;
            *(u32x4*)(T + fr * TS + 8 * cc) = *(const u32x4*)(wd + (long)(32 * s + fr) * D_ + 320 * dh + 8 * cc);
        }
        __syncthreads();
        for (int i = tid; i < 1280; i += 256) {
            int ntl = i >> 6, l = i & 63;
            int idx = l & 15, kq = l >> 4;
            int fb = 16 * (kq >> 1) + 4 * (kq & 1);
            const u16* tp = T + 16 * ntl + idx;
            u32x4 o;
            o[0] = (u32)tp[(fb + 0) * TS] | ((u32)tp[(fb + 1) * TS] << 16);
            o[1] = (u32)tp[(fb + 2) * TS] | ((u32)tp[(fb + 3) * TS] << 16);
            o[2] = (u32)tp[(fb + 8) * TS] | ((u32)tp[(fb + 9) * TS] << 16);
            o[3] = (u32)tp[(fb + 10) * TS] | ((u32)tp[(fb + 11) * TS] << 16);
            *(u32x4*)(W2p + ((long)((20 * dh + ntl) * KS2 + s) * 64 + l) * 8) = o;
        }
    }
}

__device__ __forceinline__ float wavesum(float x) {
    x = rowsum16(x);
    x += __shfl_xor(x, 16);
    x += __shfl_xor(x, 32);
    return x;
}
__device__ __forceinline__ void ln_stats(const u32 (&xv)[5], float& mb, float& rinv) {
    float sum = 0.f;
#pragma unroll
    for (int m = 0; m < 5; ++m) sum += lo2f(xv[m]) + hi2f(xv[m]);
    sum = wavesum(sum);
    float mean = sum * (1.0f / 640.0f);
    float sq = 0.f;
#pragma unroll
    for (int m = 0; m < 5; ++m) {
        float a = lo2f(xv[m]) - mean, b = hi2f(xv[m]) - mean;
        sq += a * a + b * b;
    }
    sq = wavesum(sq);
    float var = sq * (1.0f / 640.0f);
    mb = rbf(mean);
    float den = rbf(__builtin_sqrtf(rbf(rbf(var) + 1e-5f)));
    rinv = 1.0f / den;
}

__global__ __launch_bounds__(256) void ln_kernel(const u16* __restrict__ xin, const u16* __restrict__ y,
                                                 u16* __restrict__ xr, u16* __restrict__ xnp) {
    const int lane = threadIdx.x & 63;
    const int row = blockIdx.x * 4 + (threadIdx.x >> 6);
    const u32* xs = (const u32*)(xin + (long)row * D_);
    const u32* ys = (const u32*)(y + (long)row * D_);
    u32 xv[5], yv[5];
#pragma unroll
    for (int m = 0; m < 5; ++m) { xv[m] = xs[lane + 64 * m]; yv[m] = ys[lane + 64 * m]; }
    float mb, rinv;
    ln_stats(xv, mb, rinv);
    u32 x1[5];
#pragma unroll
    for (int m = 0; m < 5; ++m) {
        float a = lo2f(xv[m]), b = hi2f(xv[m]);
        float qa = rbf(rbf(a - mb) * rinv), qb = rbf(rbf(b - mb) * rinv);
        float ga = rbf(qa * lo2f(yv[m])), gb = rbf(qb * hi2f(yv[m]));
        x1[m] = pack2(a + ga, b + gb);
    }
    u32* xo = (u32*)(xr + (long)row * D_);
#pragma unroll
    for (int m = 0; m < 5; ++m) xo[lane + 64 * m] = x1[m];
    float mb2, rinv2;
    ln_stats(x1, mb2, rinv2);
    const int tb = row >> 5, tk = row & 31;
#pragma unroll
    for (int m = 0; m < 5; ++m) {
        float a = lo2f(x1[m]), b = hi2f(x1[m]);
        u32 o = pack2(rbf(a - mb2) * rinv2, rbf(b - mb2) * rinv2);
        int k = 2 * (lane + 64 * m);
        int ks = k >> 4, hh = (k >> 3) & 1, j = k & 7;
        *(u32*)(xnp + ((long)(tb * KS1 + ks) * 64 + tk + 32 * hh) * 8 + j) = o;
    }
}

constexpr int G1_STAGE = 44 * 1024;

template <int NU>
__device__ __forceinline__ void g1_compute(const char* buf, int tb, int ug, int lane, f32x16 (&acc)[4]) {
    const char* xb = buf + tb * 4096 + lane * 16;
    const char* wb = buf + (4 + 4 * ug) * 4096 + lane * 16;
    bf16x8 xf[2], wf[2][4];
    xf[0] = *(const bf16x8*)(xb);
#pragma unroll
    for (int i = 0; i < NU; ++i) wf[0][i] = *(const bf16x8*)(wb + i * 4096);
#pragma unroll
    for (int j = 0; j < 4; ++j) {
        if (j < 3) {
            xf[(j + 1) & 1] = *(const bf16x8*)(xb + (j + 1) * 1024);
#pragma unroll
            for (int i = 0; i < NU; ++i) wf[(j + 1) & 1][i] = *(const bf16x8*)(wb + i * 4096 + (j + 1) * 1024);
        }
        __builtin_amdgcn_sched_barrier(0);
#pragma unroll
        for (int i = 0; i < NU; ++i)
            acc[i] = __builtin_amdgcn_mfma_f32_32x32x16_bf16(wf[j & 1][i], xf[j & 1], acc[i], 0, 0, 0);
        __builtin_amdgcn_sched_barrier(0);
    }
}

__global__ __launch_bounds__(512, 2) void gemm1_kernel(const u16* __restrict__ xnp, const u16* __restrict__ W1p,
                                                       u16* __restrict__ hp) {
    __shared__ __attribute__((aligned(16))) char lds[3 * G1_STAGE];
    const int tid = threadIdx.x, lane = tid & 63;
    const int w = __builtin_amdgcn_readfirstlane(tid >> 6);
    const int xcd = blockIdx.x & 7, jj = blockIdx.x >> 3;
    const int rt = 8 * (xcd >> 2) + (jj & 7), ct = 4 * (xcd & 3) + (jj >> 3);
    const int tb = w & 3, ug = w >> 2;
    const int ubase = ct * 7;

    const char* src[6];
#pragma unroll
    for (int i = 0; i < 6; ++i) {
        int p = w + 8 * i;
        if (p > 43) p = 43;
        int stream = p >> 2, ksl = p & 3;
        int uu = ubase + stream - 4;
        if (uu > NUNIT - 1) uu = NUNIT - 1;
        const u16* b = (stream < 4) ? xnp + ((long)((4 * rt + stream) * KS1 + ksl) * 64 + lane) * 8
                                    : W1p + ((long)(uu * KS1 + ksl) * 64 + lane) * 8;
        src[i] = (const char*)b;
    }
#define G1_ISSUE(s) do { \
        _Pragma("unroll") for (int i = 0; i < 6; ++i) { \
            if (i < 5 || w < 4) \
                __builtin_amdgcn_global_load_lds((const u32*)(src[i] + (s) * 4096), \
                    (u32*)(lds + ((s) % 3) * G1_STAGE + (w + 8 * i) * 1024 + lane * 16), 16, 0, 0); \
        } } while (0)

    f32x16 acc[4];
#pragma unroll
    for (int a = 0; a < 4; ++a)
#pragma unroll
        for (int i = 0; i < 16; ++i) acc[a][i] = 0.f;

    G1_ISSUE(0);
    G1_ISSUE(1);
#pragma unroll
    for (int s = 0; s < 10; ++s) {
        if (s < 9) asm volatile("s_waitcnt vmcnt(5)" ::: "memory");
        else asm volatile("s_waitcnt vmcnt(0)" ::: "memory");
        __builtin_amdgcn_s_barrier();
        asm volatile("" ::: "memory");
        if (s + 2 < 10) G1_ISSUE(s + 2);
        const char* buf = lds + (s % 3) * G1_STAGE;
        if (ug == 0) g1_compute<4>(buf, tb, 0, lane, acc);
        else g1_compute<3>(buf, tb, 1, lane, acc);
        asm volatile("" ::: "memory");
    }
#undef G1_ISSUE
    const int tk = lane & 31, hh = lane >> 5;
    const int token = 128 * rt + 32 * tb + tk;
#pragma unroll
    for (int i = 0; i < 4; ++i) {
        int u = ubase + 4 * ug + i;
        if ((ug == 0 || i < 3) && u < NUNIT) {
            float hv[8];
#pragma unroll
            for (int e = 0; e < 8; ++e) {
                float g = rbf(acc[i][e]);
                float uu = rbf(acc[i][e + 8]);
                float sg = rbf(1.0f / (1.0f + __expf(-g)));
                hv[e] = sg * uu;
            }
            u32x4 o = {pack2(hv[0], hv[1]), pack2(hv[2], hv[3]), pack2(hv[4], hv[5]), pack2(hv[6], hv[7])};
            long idx = ((long)((token >> 4) * KS2 + (u >> 1)) * 64 + (token & 15) + 16 * (2 * (u & 1) + hh));
            *(u32x4*)(hp + idx * 8) = o;
        }
    }
}

constexpr int PROW = 336;
constexpr int PTILE = 64 * PROW;
constexpr int PD2 = 3;

__global__ __launch_bounds__(512, 2) void gemm2_kernel(const u16* __restrict__ hp, const u16* __restrict__ W2p,
                                                       const u16* __restrict__ xr, u16* __restrict__ xout) {
    __shared__ __attribute__((aligned(16))) char lds[4 * PTILE];
    const int tid = threadIdx.x, lane = tid & 63, w = tid >> 6;
    const int xcd = blockIdx.x & 7, jj = blockIdx.x >> 3;
    const int rt = 8 * (xcd >> 1) + (jj & 7), ct = 4 * (xcd & 1) + (jj >> 3);

    const bf16x8* hb = (const bf16x8*)hp + (long)(4 * rt) * KS2 * 64 + lane;
    const bf16x8* wbp = (const bf16x8*)W2p + (long)(5 * ct) * KS2 * 64 + lane;
    const int nsteps = (w < 6) ? 7 : 6;

    bf16x8 hf[PD2][4], wf[PD2][5];
#pragma unroll
    for (int i = 0; i < PD2; ++i) {
        int s = w + 8 * i;
#pragma unroll
        for (int m = 0; m < 4; ++m) hf[i][m] = hb[(long)(m * KS2 + s) * 64];
#pragma unroll
        for (int n = 0; n < 5; ++n) wf[i][n] = wbp[(long)(n * KS2 + s) * 64];
    }
    u32x4 res0, res1;
    {
        int t0 = tid;
        res0 = *(const u32x4*)(xr + (long)(64 * rt + t0 / 10) * D_ + 80 * ct + 8 * (t0 % 10));
        int t1 = tid + 512;
        if (t1 < 640) res1 = *(const u32x4*)(xr + (long)(64 * rt + t1 / 10) * D_ + 80 * ct + 8 * (t1 % 10));
    }
    f32x4 acc[5][4];
#pragma unroll
    for (int n = 0; n < 5; ++n)
#pragma unroll
        for (int m = 0; m < 4; ++m) acc[n][m] = (f32x4){0.f, 0.f, 0.f, 0.f};
#pragma unroll
    for (int i = 0; i < 7; ++i) {
        if (i < nsteps) {
#pragma unroll
            for (int n = 0; n < 5; ++n)
#pragma unroll
                for (int m = 0; m < 4; ++m)
                    acc[n][m] = __builtin_amdgcn_mfma_f32_16x16x32_bf16(wf[i % PD2][n], hf[i % PD2][m], acc[n][m], 0, 0, 0);
            if (i + PD2 < nsteps) {
                int s = w + 8 * (i + PD2);
#pragma unroll
                for (int m = 0; m < 4; ++m) hf[i % PD2][m] = hb[(long)(m * KS2 + s) * 64];
#pragma unroll
                for (int n = 0; n < 5; ++n) wf[i % PD2][n] = wbp[(long)(n * KS2 + s) * 64];
            }
        }
    }
    const int tl = lane & 15, q = lane >> 4;
    char* slot = lds + (w & 3) * PTILE + tl * PROW + q * 16;
    if (w >= 4) {
#pragma unroll
        for (int n = 0; n < 5; ++n)
#pragma unroll
            for (int m = 0; m < 4; ++m) *(f32x4*)(slot + m * 16 * PROW + n * 64) = acc[n][m];
    }
    __syncthreads();
    if (w < 4) {
#pragma unroll
        for (int n = 0; n < 5; ++n)
#pragma unroll
            for (int m = 0; m < 4; ++m) {
                f32x4 o = *(const f32x4*)(slot + m * 16 * PROW + n * 64);
                *(f32x4*)(slot + m * 16 * PROW + n * 64) = o + acc[n][m];
            }
    }
    __syncthreads();
#pragma unroll
    for (int it = 0; it < 2; ++it) {
        int t = tid + 512 * it;
        if (t < 640) {
            int tok = t / 10, cc = t % 10;
            const char* p = lds + tok * PROW + cc * 32;
            f32x4 s0 = *(const f32x4*)(p), s1 = *(const f32x4*)(p + 16);
#pragma unroll
            for (int k = 1; k < 4; ++k) {
                s0 += *(const f32x4*)(p + k * PTILE);
                s1 += *(const f32x4*)(p + k * PTILE + 16);
            }
            u32x4 rs = it ? res1 : res0;
            u32x4 o;
            o[0] = pack2(lo2f(rs[0]) + rbf(s0[0]), hi2f(rs[0]) + rbf(s0[1]));
            o[1] = pack2(lo2f(rs[1]) + rbf(s0[2]), hi2f(rs[1]) + rbf(s0[3]));
            o[2] = pack2(lo2f(rs[2]) + rbf(s1[0]), hi2f(rs[2]) + rbf(s1[1]));
            o[3] = pack2(lo2f(rs[3]) + rbf(s1[2]), hi2f(rs[3]) + rbf(s1[3]));
            *(u32x4*)(xout + (long)(64 * rt + tok) * D_ + 80 * ct + 8 * cc) = o;
        }
    }
}

extern "C" void kernel_launch(void* const* d_in, const int* in_sizes, int n_in,
                              void* d_out, int out_size, void* d_ws, size_t ws_size,
                              hipStream_t stream) {
    const u16* q = (const u16*)d_in[0];
    const u16* k = (const u16*)d_in[1];
    const u16* v = (const u16*)d_in[2];
    const u16* sk = (const u16*)d_in[3];
    const u16* ssmk = (const u16*)d_in[4];
    const u16* ssmD = (const u16*)d_in[5];
    const u16* wg = (const u16*)d_in[7];
    const u16* wu = (const u16*)d_in[8];
    const u16* wd = (const u16*)d_in[9];
    u16* out = (u16*)d_out;

    char* ws = (char*)d_ws;
    constexpr size_t SZ_X = (size_t)S_ * D_ * 2;
    u16* kvR = (u16*)(ws);
    u16* y = (u16*)(ws + SZ_X);
    u16* xr = (u16*)(ws + 2 * SZ_X);
    u16* x2 = (u16*)(ws + 3 * SZ_X);
    u16* W1p = (u16*)(ws + 4 * SZ_X);
    u16* W2p = (u16*)(ws + 4 * SZ_X + (size_t)NUNIT * KS1 * 1024);
    u16* hp = (u16*)(ws + 4 * SZ_X + (size_t)NUNIT * KS1 * 1024 + (size_t)40 * KS2 * 1024);
    u16* xnp = (u16*)(ws + 4 * SZ_X + (size_t)NUNIT * KS1 * 1024 + (size_t)40 * KS2 * 1024 + (size_t)128 * KS2 * 1024);

    prep_kernel<<<320, 256, 0, stream>>>(k, v, sk, kvR);
    conv_kernel<<<NCONV + NREP1 + NREP2, 256, 0, stream>>>(kvR, ssmk, ssmD, y, wg, wu, wd, W1p, W2p);
    ln_kernel<<<512, 256, 0, stream>>>(q, y, xr, xnp);
    gemm1_kernel<<<256, 512, 0, stream>>>(xnp, W1p, hp);
    gemm2_kernel<<<256, 512, 0, stream>>>(hp, W2p, xr, x2);
    ln_kernel<<<512, 256, 0, stream>>>(x2, y, xr, xnp);
    gemm1_kernel<<<256, 512, 0, stream>>>(xnp, W1p, hp);
    gemm2_kernel<<<256, 512, 0, stream>>>(hp, W2p, xr, out);
}
```

```cpp
#include <hip/hip_runtime.h>
#include <hip/hip_bf16.h>
#include <stdint.h>

typedef unsigned short u16;
typedef unsigned int u32;
typedef __attribute__((ext_vector_type(8))) short bf16x8;
typedef __attribute__((ext_vector_type(4))) float f32x4;
typedef __attribute__((ext_vector_type(16))) float f32x16;
typedef __attribute__((ext_vector_type(4))) u32 u32x4;
typedef __attribute__((ext_vector_type(2))) u32 u32x2;
typedef __attribute__((ext_vector_type(2))) float f32x2;
typedef __attribute__((ext_vector_type(2))) __bf16 bf16x2v;

constexpr int S_ = 2048, D_ = 640, F_ = 1728;
constexpr int NUNIT = 108;
constexpr int KS1 = 40;
constexpr int KS2 = 54;

__device__ __forceinline__ float lo2f(u32 v) { return __uint_as_float(v << 16); }
__device__ __forceinline__ float hi2f(u32 v) { return __uint_as_float(v & 0xffff0000u); }
__device__ __forceinline__ u32 pack2(float a, float b) {
    f32x2 f = {a, b};
    bf16x2v r = __builtin_convertvector(f, bf16x2v);
    return __builtin_bit_cast(u32, r);
}
__device__ __forceinline__ float rbf(float a) {
    __bf16 b = (__bf16)a;
    return __uint_as_float(((u32)__builtin_bit_cast(u16, b)) << 16);
}
__device__ __forceinline__ u16 f2bf(float a) {
    __bf16 b = (__bf16)a;
    return __builtin_bit_cast(u16, b);
}

__device__ __forceinline__ float rowsum16(float x) {
    x += __builtin_bit_cast(float, __builtin_amdgcn_update_dpp(0, __builtin_bit_cast(int, x), 0x128, 0xf, 0xf, false));
    x += __builtin_bit_cast(float, __builtin_amdgcn_update_dpp(0, __builtin_bit_cast(int, x), 0x124, 0xf, 0xf, false));
    x += __builtin_bit_cast(float, __builtin_amdgcn_update_dpp(0, __builtin_bit_cast(int, x), 0x122, 0xf, 0xf, false));
    x += __builtin_bit_cast(float, __builtin_amdgcn_update_dpp(0, __builtin_bit_cast(int, x), 0x121, 0xf, 0xf, false));
    return x;
}

__device__ __forceinline__ float wavesum(float x) {
    x = rowsum16(x);
    x += __shfl_xor(x, 16);
    x += __shfl_xor(x, 32);
    return x;
}
__device__ __forceinline__ long xf_idx(int token, int d) {
    return ((long)((token >> 5) * 80 + (d >> 3)) * 32 + (token & 31)) * 8 + (d & 7);
}
__device__ __forceinline__ float dot2bf(u32 a, u32 b, float c) {
    return __builtin_amdgcn_fdot2_f32_bf16(__builtin_bit_cast(bf16x2v, a), __builtin_bit_cast(bf16x2v, b), c, false);
}
__device__ __forceinline__ void ln_finish(float mean, float var, float& mb, float& rinv) {
    mb = rbf(mean);
    float den = rbf(__builtin_sqrtf(rbf(rbf(var) + 1e-5f)));
    rinv = 1.0f / den;
}
constexpr u32 GTAG = 0x13572468u;
constexpr int GRAN_WORDS = 32 * 8 * 2 * 64;

__global__ __launch_bounds__(256) void prep_kernel(const u16* __restrict__ k, const u16* __restrict__ v,
                                                   const u16* __restrict__ sk, u16* __restrict__ kvR,
                                                   const u16* __restrict__ q, u16* __restrict__ qF,
                                                   f32x2* __restrict__ qs, u32x4* __restrict__ gran) {
    __shared__ __attribute__((aligned(16))) u32 T[64 * 32];
    if (blockIdx.x >= 320) {
        const int b = blockIdx.x - 320;
        if (b < 64) { u32x4 z = {0u, 0u, 0u, 0u}; gran[b * 256 + threadIdx.x] = z; }
        const int lane = threadIdx.x & 63;
        const int row = b * 4 + (threadIdx.x >> 6);
        const u32* xs = (const u32*)(q + (long)row * D_);
        u32 xv[5];
#pragma unroll
        for (int m = 0; m < 5; ++m) xv[m] = xs[lane + 64 * m];
        float sum = 0.f;
#pragma unroll
        for (int m = 0; m < 5; ++m) sum += lo2f(xv[m]) + hi2f(xv[m]);
        sum = wavesum(sum);
        float mean = sum * (1.0f / 640.0f);
        float sq = 0.f;
#pragma unroll
        for (int m = 0; m < 5; ++m) {
            float a = lo2f(xv[m]) - mean, c = hi2f(xv[m]) - mean;
            sq += a * a + c * c;
        }
        sq = wavesum(sq);
        float mb, rinv;
        ln_finish(mean, sq * (1.0f / 640.0f), mb, rinv);
        if (lane == 0) { f32x2 o = {mb, rinv}; qs[row] = o; }
#pragma unroll
        for (int m = 0; m < 5; ++m) *(u32*)(qF + xf_idx(row, 2 * (lane + 64 * m))) = xv[m];
        return;
    }
    const int st = blockIdx.x / 10, dt = blockIdx.x % 10;
    const int s0 = st * 64, d0 = dt * 64;
    const int t = threadIdx.x, dc = t & 7, rp = t >> 3;
    const int s = s0 + 2 * rp, d = d0 + 8 * dc;
    u32x4 kr[5];
#pragma unroll
    for (int i = 0; i < 5; ++i) {
        int row = s - 3 + i;
        u32x4 z = {0u, 0u, 0u, 0u};
        kr[i] = (row >= 0) ? *(const u32x4*)(k + (long)row * D_ + d) : z;
    }
    u32x4 v0 = *(const u32x4*)(v + (long)s * D_ + d);
    u32x4 v1 = *(const u32x4*)(v + (long)(s + 1) * D_ + d);
    u32x4 skv[4];
#pragma unroll
    for (int i = 0; i < 4; ++i) skv[i] = *(const u32x4*)(sk + (long)d * 4 + 8 * i);
    const int p = 31 - rp;
#pragma unroll
    for (int e = 0; e < 8; ++e) {
        u32 w01 = skv[e >> 1][(e & 1) * 2 + 0], w23 = skv[e >> 1][(e & 1) * 2 + 1];
        float c0 = lo2f(w01), c1 = hi2f(w01), c2 = lo2f(w23), c3 = hi2f(w23);
        float kk[5];
#pragma unroll
        for (int i = 0; i < 5; ++i) {
            u32 wv = kr[i][e >> 1];
            kk[i] = (e & 1) ? hi2f(wv) : lo2f(wv);
        }
        float va = (e & 1) ? hi2f(v0[e >> 1]) : lo2f(v0[e >> 1]);
        float vb = (e & 1) ? hi2f(v1[e >> 1]) : lo2f(v1[e >> 1]);
        float a = rbf(kk[0] * c3);
        a = rbf(a + rbf(kk[1] * c2));
        a = rbf(a + rbf(kk[2] * c1));
        a = rbf(a + rbf(kk[3] * c0));
        float b = rbf(kk[1] * c3);
        b = rbf(b + rbf(kk[2] * c2));
        b = rbf(b + rbf(kk[3] * c1));
        b = rbf(b + rbf(kk[4] * c0));
        u32 pk = pack2(b * vb, a * va);
        int dl = 8 * dc + e;
        T[dl * 32 + ((((p >> 2) ^ dc)) << 2) + (p & 3)] = pk;
    }
    __syncthreads();
#pragma unroll
    for (int it = 0; it < 2; ++it) {
        int idx = t + 256 * it;
        int dl = idx >> 3, c = idx & 7;
        u32x4 o = *(const u32x4*)&T[dl * 32 + ((c ^ (dl >> 3)) << 2)];
        *(u32x4*)(kvR + (long)(d0 + dl) * S_ + (1984 - s0) + 8 * c) = o;
    }
}

constexpr int HC_STRIDE = 4416;
constexpr int KV_BYTES = 47 * 144;
constexpr int WAVE_LDS = 4 * HC_STRIDE + KV_BYTES;
constexpr int YBUF_OFF = 4 * WAVE_LDS;
constexpr int CONV_LDS = YBUF_OFF + 2048 * 8;
constexpr int NCONV = 160;
constexpr int NREP1 = 160;
constexpr int NREP2 = 108;
constexpr int NCS = 54;

__global__ __launch_bounds__(256, 1) void conv_kernel(const u16* __restrict__ kvR, const u16* __restrict__ ssmk,
                                                      const u16* __restrict__ ssmD, u16* __restrict__ yF,
                                                      const u16* __restrict__ wg, const u16* __restrict__ wu,
                                                      const u16* __restrict__ wd, u16* __restrict__ W1p,
                                                      u16* __restrict__ W2p, const u16* __restrict__ qF,
                                                      const f32x2* __restrict__ qs, u16* __restrict__ xaF,
                                                      float* __restrict__ cs) {
    __shared__ __attribute__((aligned(16))) char lds[CONV_LDS];
    const int tid = threadIdx.x, lane = tid & 63, w = tid >> 6;
    const int bid = blockIdx.x;
    if (bid < NCONV) {
        const int ch = bid * 4 + w;
        char* wl = lds + w * WAVE_LDS;
        char* kvl = wl + 4 * HC_STRIDE;
        const u32x4* hsrc = (const u32x4*)(ssmk + (long)ch * S_);
        const u32x4* ksrc = (const u32x4*)(kvR + (long)ch * S_);
        u32x4 kvv[4];
#pragma unroll
        for (int m = 0; m < 4; ++m) kvv[m] = ksrc[lane + 64 * m];
#pragma unroll
        for (int m = 0; m < 5; ++m) {
            int c = lane - 8 + 64 * m;
            if (c < 256) {
                u32x4 z = {0u, 0u, 0u, 0u};
                u32x4 a = (c >= 0) ? hsrc[c] : z;
                u32x4 b = (c + 1 >= 0 && c + 1 < 256) ? hsrc[c + 1] : z;
                u32 ww[8] = {a[0], a[1], a[2], a[3], b[0], b[1], b[2], b[3]};
                u32x4 o0, o1, o2, o3;
#pragma unroll
                for (int i = 0; i < 4; ++i) {
                    o0[i] = __builtin_amdgcn_alignbit(ww[i + 1], ww[i], 16);
                    o1[i] = ww[i + 1];
                    o2[i] = __builtin_amdgcn_alignbit(ww[i + 2], ww[i + 1], 16);
                    o3[i] = ww[i + 2];
                }
                char* dst = wl + (c + 8) * 16;
                *(u32x4*)(dst + 0 * HC_STRIDE) = o0;
                *(u32x4*)(dst + 1 * HC_STRIDE) = o1;
                *(u32x4*)(dst + 2 * HC_STRIDE) = o2;
                *(u32x4*)(dst + 3 * HC_STRIDE) = o3;
            }
        }
#pragma unroll
        for (int m = 0; m < 4; ++m) {
            int c = lane + 64 * m;
            *(u32x4*)(kvl + (c >> 3) * 144 + (c & 7) * 16) = kvv[m];
        }
        {
            u32x4 z = {0u, 0u, 0u, 0u};
#pragma unroll
            for (int m = 0; m < 3; ++m) {
                int c = lane + 64 * m;
                if (c < 135) *(u32x4*)(kvl + 32 * 144 + c * 16) = z;
            }
        }
        __builtin_amdgcn_s_waitcnt(0xC07F);
        __builtin_amdgcn_wave_barrier();
        u32x2 qv[8];
        f32x2 qst[8];
#pragma unroll
        for (int it = 0; it < 8; ++it) {
            int s = tid + 256 * it;
            qv[it] = *(const u32x2*)(qF + xf_idx(s, bid * 4));
            qst[it] = qs[s];
        }

        const int r = lane & 15, kq = lane >> 4;
        u32 aoffA = (u32)(w * WAVE_LDS + (r & 3) * HC_STRIDE + 2 * ((r & 12) + 8 * kq));
        u32 aoffB = aoffA + 8, aoffC = aoffA + 32, aoffD = aoffA + 40;
        asm volatile("" : "+v"(aoffB));
        asm volatile("" : "+v"(aoffC));
        asm volatile("" : "+v"(aoffD));
        const u32 boff = (u32)(w * WAVE_LDS + 4 * HC_STRIDE + 16 * (9 * (31 - r) + kq));

        f32x4 acc[2][4];
#pragma unroll
        for (int n = 0; n < 2; ++n)
#pragma unroll
            for (int m = 0; m < 4; ++m) acc[n][m] = (f32x4){0.f, 0.f, 0.f, 0.f};

        bf16x8 A[136], B0[32], B1[64];
#define LOADA(i) do { \
            u32x2 l0 = *(const u32x2*)(lds + (((i) & 1) ? aoffC : aoffA) + 64 * ((i) >> 1)); \
            u32x2 l1 = *(const u32x2*)(lds + (((i) & 1) ? aoffD : aoffB) + 64 * ((i) >> 1)); \
            u32x4 t4 = {l0[0], l0[1], l1[0], l1[1]}; \
            A[i] = __builtin_bit_cast(bf16x8, t4); } while (0)
#define KSO(ks) (16 * (9 * ((ks) >> 1) + 4 * ((ks) & 1)))
#define LOADB(ks) do { \
            B1[ks] = *(const bf16x8*)(lds + boff + KSO(ks) - 16 * 9 * 16); \
            if ((ks) < 32) B0[(ks) < 32 ? (ks) : 0] = *(const bf16x8*)(lds + boff + KSO(ks)); } while (0)
#pragma unroll
        for (int i = 0; i < 6; ++i) LOADA(i);
        LOADB(0);
        LOADB(1);
        __builtin_amdgcn_sched_barrier(0);
#pragma unroll
        for (int ks = 0; ks < 64; ++ks) {
            if (ks + 2 < 64) {
                LOADA(2 * ks + 6);
                LOADA(2 * ks + 7);
                LOADB(ks + 2);
            }
            __builtin_amdgcn_sched_barrier(0);
#pragma unroll
            for (int m = 0; m < 4; ++m)
                acc[1][m] = __builtin_amdgcn_mfma_f32_16x16x32_bf16(A[2 * ks + m], B1[ks], acc[1][m], 0, 0, 0);
            if (ks < 32) {
#pragma unroll
                for (int m = 0; m < 4; ++m)
                    acc[0][m] = __builtin_amdgcn_mfma_f32_16x16x32_bf16(A[2 * ks + m], B0[ks < 32 ? ks : 0], acc[0][m], 0, 0, 0);
            }
            __builtin_amdgcn_sched_barrier(0);
        }
#undef LOADA
#undef LOADB
#undef KSO
        const float Dv = lo2f((u32)ssmD[ch]);
        u16* ybuf = (u16*)(lds + YBUF_OFF);
#pragma unroll
        for (int n = 0; n < 2; ++n)
#pragma unroll
            for (int m = 0; m < 4; ++m) {
                int a = 16 * n + r;
                int ib = 60 - 16 * m - 4 * kq;
                u32x2 kk = *(const u32x2*)(kvl + (31 - a) * 144 + ib * 2);
                float kvf[4] = {hi2f(kk[1]), lo2f(kk[1]), hi2f(kk[0]), lo2f(kk[0])};
                int sb = 64 * a + 16 * m + 4 * kq;
#pragma unroll
                for (int j = 0; j < 4; ++j) {
                    float yc = rbf(acc[n][m][j]);
                    float tt = rbf(kvf[j] * Dv);
                    ybuf[(sb + j) * 4 + w] = f2bf(yc + tt);
                }
            }
        __syncthreads();
#pragma unroll
        for (int it = 0; it < 8; ++it) {
            int s = tid + 256 * it;
            u32x2 yo = *(const u32x2*)(ybuf + s * 4);
            float mb = qst[it][0], rinv = qst[it][1];
            u32x2 xo;
#pragma unroll
            for (int j = 0; j < 2; ++j) {
                float a = lo2f(qv[it][j]), b = hi2f(qv[it][j]);
                float qa = rbf(rbf(a - mb) * rinv), qb = rbf(rbf(b - mb) * rinv);
                float ga = rbf(qa * lo2f(yo[j])), gb = rbf(qb * hi2f(yo[j]));
                xo[j] = pack2(a + ga, b + gb);
            }
            long idx = xf_idx(s, bid * 4);
            *(u32x2*)(yF + idx) = yo;
            *(u32x2*)(xaF + idx) = xo;
        }
    } else if (bid < NCONV + NREP1) {
        const int b = bid - NCONV;
        const int ks = b >> 2, ug = b & 3;
        u16* T = (u16*)lds;
        constexpr int TS = 440;
        for (int i = tid; i < 1728; i += 256) {
            int mat = i / 864, rem = i % 864, kr = rem / 54, cc = rem % 54;
            const u16* src = (mat ? wu : wg) + (long)(16 * ks + kr) * F_ + 432 * ug + 8 * cc;
            *(u32x4*)(T + (mat * 16 + kr) * TS + 8 * cc) = *(const u32x4*)src;
        }
        __syncthreads();
        for (int i = tid; i < 1728; i += 256) {
            int uu = i >> 6, l = i & 63;
            int ii = l & 31, hh = l >> 5;
            int mat = ii >> 4, col = 16 * uu + (ii & 15);
            const u16* tp = T + (mat * 16 + 8 * hh) * TS + col;
            u32x4 o;
#pragma unroll
            for (int j = 0; j < 4; ++j) o[j] = (u32)tp[(2 * j) * TS] | ((u32)tp[(2 * j + 1) * TS] << 16);
            *(u32x4*)(W1p + ((long)((27 * ug + uu) * KS1 + ks) * 64 + l) * 8) = o;
        }
    } else if (bid >= NCONV + NREP1 + NREP2) {
        const int b = bid - NCONV - NREP1 - NREP2;
        const int mat = b / 27, cb = b % 27;
        const u16* wsrc = mat ? wu : wg;
        const int cp = tid & 31, kg = tid >> 5;
        float a0 = 0.f, a1 = 0.f;
#pragma unroll 16
        for (int i = 0; i < 80; ++i) {
            u32 wv = *(const u32*)(wsrc + (long)(kg * 80 + i) * F_ + cb * 64 + 2 * cp);
            a0 += lo2f(wv);
            a1 += hi2f(wv);
        }
        float* T = (float*)lds;
        T[kg * 64 + 2 * cp] = a0;
        T[kg * 64 + 2 * cp + 1] = a1;
        __syncthreads();
        if (tid < 64) {
            float t = 0.f;
#pragma unroll
            for (int g = 0; g < 8; ++g) t += T[g * 64 + tid];
            int f = cb * 64 + tid;
            cs[(f >> 4) * 32 + mat * 16 + (f & 15)] = t;
        }
    } else {
        const int b = bid - NCONV - NREP1;
        const int s = b >> 1, dh = b & 1;
        u16* T = (u16*)lds;
        constexpr int TS = 328;
        for (int i = tid; i < 1280; i += 256) {
            int fr = i / 40, cc = i % 40;
            *(u32x4*)(T + fr * TS + 8 * cc) = *(const u32x4*)(wd + (long)(32 * s + fr) * D_ + 320 * dh + 8 * cc);
        }
        __syncthreads();
        for (int i = tid; i < 1280; i += 256) {
            int ntl = i >> 6, l = i & 63;
            int idx = l & 15, kq = l >> 4;
            int fb = 16 * (kq >> 1) + 4 * (kq & 1);
            const u16* tp = T + 16 * ntl + idx;
            u32x4 o;
            o[0] = (u32)tp[(fb + 0) * TS] | ((u32)tp[(fb + 1) * TS] << 16);
            o[1] = (u32)tp[(fb + 2) * TS] | ((u32)tp[(fb + 3) * TS] << 16);
            o[2] = (u32)tp[(fb + 8) * TS] | ((u32)tp[(fb + 9) * TS] << 16);
            o[3] = (u32)tp[(fb + 10) * TS] | ((u32)tp[(fb + 11) * TS] << 16);
            *(u32x4*)(W2p + ((long)((20 * dh + ntl) * KS2 + s) * 64 + l) * 8) = o;
        }
    }
}

constexpr int G1_STAGE = 44 * 1024;
constexpr int G1_CS_OFF = 3 * G1_STAGE;

template <int NU>
__device__ __forceinline__ void g1_main(char* lds, const char* base, const u32 (&src)[6], int w, int tb, int ug, int lane,
                                        f32x16 (&acc)[4], float& s1, float& s2) {
#define G1_ISSUE(s) do { \
        _Pragma("unroll") for (int i = 0; i < 6; ++i) { \
            if (i < 5 || NU == 4) \
                __builtin_amdgcn_global_load_lds((const u32*)(base + (src[i] + (u32)((s) * 4096))), \
                    (u32*)(lds + ((s) % 3) * G1_STAGE + (w + 8 * i) * 1024 + lane * 16), 16, 0, 0); \
        } } while (0)
    G1_ISSUE(0);
    G1_ISSUE(1);
    bf16x8 xf[4], wf[4][NU];
#pragma unroll
    for (int s = 0; s <= 10; ++s) {
        if (s < 10) {
            if (s < 9) asm volatile("s_waitcnt vmcnt(5) lgkmcnt(0)" ::: "memory");
            else asm volatile("s_waitcnt vmcnt(0) lgkmcnt(0)" ::: "memory");
            __builtin_amdgcn_s_barrier();
            asm volatile("" ::: "memory");
            if (s + 2 < 10) G1_ISSUE(s + 2);
        }
        const char* xb = lds + (s % 3) * G1_STAGE + tb * 4096 + lane * 16;
        const char* wb = lds + (s % 3) * G1_STAGE + (4 + 4 * ug) * 4096 + lane * 16;
#pragma unroll
        for (int j = 0; j < 4; ++j) {
            __builtin_amdgcn_sched_barrier(0);
            if (s > 0) {
#pragma unroll
                for (int i = 0; i < NU; ++i)
                    acc[i] = __builtin_amdgcn_mfma_f32_32x32x16_bf16(wf[j][i], xf[j], acc[i], 0, 0, 0);
                u32x4 xx = __builtin_bit_cast(u32x4, xf[j]);
#pragma unroll
                for (int e = 0; e < 4; ++e) {
                    s1 = dot2bf(xx[e], 0x3f803f80u, s1);
                    s2 = dot2bf(xx[e], xx[e], s2);
                }
            }
            __builtin_amdgcn_sched_barrier(0);
            if (s < 10) {
                xf[j] = *(const bf16x8*)(xb + j * 1024);
#pragma unroll
                for (int i = 0; i < NU; ++i) wf[j][i] = *(const bf16x8*)(wb + i * 4096 + j * 1024);
            }
        }
        __builtin_amdgcn_sched_barrier(0);
    }
#undef G1_ISSUE
}

__global__ __launch_bounds__(512, 2) void gemm1_kernel(const u16* __restrict__ xF, const u16* __restrict__ W1p,
                                                       const float* __restrict__ cs, u16* __restrict__ hp) {
    __shared__ __attribute__((aligned(16))) char lds[3 * G1_STAGE + 1024];
    const int tid = threadIdx.x, lane = tid & 63;
    const int w = __builtin_amdgcn_readfirstlane(tid >> 6);
    const int xcd = blockIdx.x & 7, jj = blockIdx.x >> 3;
    const int rt = 8 * (xcd >> 2) + (jj & 7), ct = 4 * (xcd & 3) + (jj >> 3);
    const int tb = w & 3, ug = w >> 2;
    const int ubase = ct * 7;

    const char* base = (const char*)W1p;
    const u32 xoff = (u32)((const char*)xF - base);
    u32 src[6];
#pragma unroll
    for (int i = 0; i < 6; ++i) {
        int p = w + 8 * i;
        if (p > 43) p = 43;
        int stream = p >> 2, ksl = p & 3;
        int uu = ubase + stream - 4;
        if (uu > NUNIT - 1) uu = NUNIT - 1;
        src[i] = (stream < 4) ? xoff + (u32)((((4 * rt + stream) * KS1 + ksl) * 64 + lane) * 16)
                              : (u32)(((uu * KS1 + ksl) * 64 + lane) * 16);
    }
    float csv = 0.f;
    if (tid < 224) {
        int e = ubase * 32 + tid;
        csv = (e < NUNIT * 32) ? cs[e] : 0.f;
    }
    f32x16 acc[4];
#pragma unroll
    for (int a = 0; a < 4; ++a)
#pragma unroll
        for (int i = 0; i < 16; ++i) acc[a][i] = 0.f;
    float s1 = 0.f, s2 = 0.f;
    if (tid < 224) *(float*)(lds + G1_CS_OFF + tid * 4) = csv;
    if (ug == 0) g1_main<4>(lds, base, src, w, tb, 0, lane, acc, s1, s2);
    else g1_main<3>(lds, base, src, w, tb, 1, lane, acc, s1, s2);

    s1 += __shfl_xor(s1, 32);
    s2 += __shfl_xor(s2, 32);
    const float mean = s1 * (1.0f / 640.0f);
    float var = s2 * (1.0f / 640.0f) - mean * mean;
    var = var > 0.f ? var : 0.f;
    float mb, rinv;
    ln_finish(mean, var, mb, rinv);
    const int tk = lane & 31, hh = lane >> 5;
    const int token = 128 * rt + 32 * tb + tk;
#pragma unroll
    for (int i = 0; i < 4; ++i) {
        int u = ubase + 4 * ug + i;
        if ((ug == 0 || i < 3) && u < NUNIT) {
            const char* cp = lds + G1_CS_OFF + (4 * ug + i) * 128 + hh * 16;
            f32x4 c0 = *(const f32x4*)(cp), c1 = *(const f32x4*)(cp + 32);
            f32x4 c2 = *(const f32x4*)(cp + 64), c3 = *(const f32x4*)(cp + 96);
            float hv[8];
#pragma unroll
            for (int e = 0; e < 8; ++e) {
                float cg = (e < 4) ? c0[e & 3] : c1[e & 3];
                float cu = (e < 4) ? c2[e & 3] : c3[e & 3];
                float g = rbf((acc[i][e] - mb * cg) * rinv);
                float uu = rbf((acc[i][e + 8] - mb * cu) * rinv);
                float sg = rbf(1.0f / (1.0f + __expf(-g)));
                hv[e] = sg * uu;
            }
            u32x4 o = {pack2(hv[0], hv[1]), pack2(hv[2], hv[3]), pack2(hv[4], hv[5]), pack2(hv[6], hv[7])};
            long idx = ((long)((token >> 4) * KS2 + (u >> 1)) * 64 + (token & 15) + 16 * (2 * (u & 1) + hh));
            *(u32x4*)(hp + idx * 8) = o;
        }
    }
}

constexpr int PROW = 336;
constexpr int PTILE = 64 * PROW;
constexpr int PD2 = 3;
typedef __attribute__((address_space(1))) unsigned long long gu64;

template <bool MID>
__global__ __launch_bounds__(512, 2) void gemm2_kernel(const u16* __restrict__ hp, const u16* __restrict__ W2p,
                                                       const u16* __restrict__ xresF, const u16* __restrict__ yF,
                                                       u16* __restrict__ xout, unsigned long long* gran) {
    __shared__ __attribute__((aligned(16))) char lds[4 * PTILE];
    const int tid = threadIdx.x, lane = tid & 63, w = tid >> 6;
    const int xcd = blockIdx.x & 7, jj = blockIdx.x >> 3;
    const int rt = 8 * (xcd >> 1) + (jj & 7), ct = 4 * (xcd & 1) + (jj >> 3);

    const bf16x8* hb = (const bf16x8*)hp + (long)(4 * rt) * KS2 * 64 + lane;
    const bf16x8* wbp = (const bf16x8*)W2p + (long)(5 * ct) * KS2 * 64 + lane;
    const int nsteps = (w < 6) ? 7 : 6;

    bf16x8 hf[PD2][4], wf[PD2][5];
#pragma unroll
    for (int i = 0; i < PD2; ++i) {
        int s = w + 8 * i;
#pragma unroll
        for (int m = 0; m < 4; ++m) hf[i][m] = hb[(long)(m * KS2 + s) * 64];
#pragma unroll
        for (int n = 0; n < 5; ++n) wf[i][n] = wbp[(long)(n * KS2 + s) * 64];
    }
    const int tok = lane;
    const int token = 64 * rt + tok;
    u32x4 res[2], yv[2];
#pragma unroll
    for (int it = 0; it < 2; ++it) {
        int cc = w + 8 * it;
        if (cc < 10) {
            long idx = xf_idx(token, 80 * ct + 8 * cc);
            res[it] = *(const u32x4*)(xresF + idx);
            if (MID) yv[it] = *(const u32x4*)(yF + idx);
        }
    }
    f32x4 acc[5][4];
#pragma unroll
    for (int n = 0; n < 5; ++n)
#pragma unroll
        for (int m = 0; m < 4; ++m) acc[n][m] = (f32x4){0.f, 0.f, 0.f, 0.f};
#pragma unroll
    for (int i = 0; i < 7; ++i) {
        if (i < nsteps) {
#pragma unroll
            for (int n = 0; n < 5; ++n)
#pragma unroll
                for (int m = 0; m < 4; ++m)
                    acc[n][m] = __builtin_amdgcn_mfma_f32_16x16x32_bf16(wf[i % PD2][n], hf[i % PD2][m], acc[n][m], 0, 0, 0);
            if (i + PD2 < nsteps) {
                int s = w + 8 * (i + PD2);
#pragma unroll
                for (int m = 0; m < 4; ++m) hf[i % PD2][m] = hb[(long)(m * KS2 + s) * 64];
#pragma unroll
                for (int n = 0; n < 5; ++n) wf[i % PD2][n] = wbp[(long)(n * KS2 + s) * 64];
            }
        }
    }
    const int tl = lane & 15, q = lane >> 4;
    char* slot = lds + (w & 3) * PTILE + tl * PROW + q * 16;
    if (w >= 4) {
#pragma unroll
        for (int n = 0; n < 5; ++n)
#pragma unroll
            for (int m = 0; m < 4; ++m) *(f32x4*)(slot + m * 16 * PROW + n * 64) = acc[n][m];
    }
    __syncthreads();
    if (w < 4) {
#pragma unroll
        for (int n = 0; n < 5; ++n)
#pragma unroll
            for (int m = 0; m < 4; ++m) {
                f32x4 o = *(const f32x4*)(slot + m * 16 * PROW + n * 64);
                *(f32x4*)(slot + m * 16 * PROW + n * 64) = o + acc[n][m];
            }
    }
    __syncthreads();
    u32x4 x2[2];
#pragma unroll
    for (int it = 0; it < 2; ++it) {
        int cc = w + 8 * it;
        if (cc < 10) {
            const char* p = lds + tok * PROW + cc * 32;
            f32x4 s0 = *(const f32x4*)(p), s1 = *(const f32x4*)(p + 16);
#pragma unroll
            for (int k = 1; k < 4; ++k) {
                s0 += *(const f32x4*)(p + k * PTILE);
                s1 += *(const f32x4*)(p + k * PTILE + 16);
            }
            u32x4 rs = res[it];
            x2[it][0] = pack2(lo2f(rs[0]) + rbf(s0[0]), hi2f(rs[0]) + rbf(s0[1]));
            x2[it][1] = pack2(lo2f(rs[1]) + rbf(s0[2]), hi2f(rs[1]) + rbf(s0[3]));
            x2[it][2] = pack2(lo2f(rs[2]) + rbf(s1[0]), hi2f(rs[2]) + rbf(s1[1]));
            x2[it][3] = pack2(lo2f(rs[3]) + rbf(s1[2]), hi2f(rs[3]) + rbf(s1[3]));
            if (!MID) *(u32x4*)(xout + (long)token * D_ + 80 * ct + 8 * cc) = x2[it];
        }
    }
    if (MID) {
        __syncthreads();
        float* red = (float*)lds;
        float* red2 = (float*)(lds + 8192);
#pragma unroll
        for (int it = 0; it < 2; ++it) {
            int cc = w + 8 * it;
            if (cc < 10) {
                float a = 0.f, b = 0.f;
#pragma unroll
                for (int e = 0; e < 4; ++e) { a = dot2bf(x2[it][e], 0x3f803f80u, a); b = dot2bf(x2[it][e], x2[it][e], b); }
                red[cc * 64 + tok] = a;
                red[640 + cc * 64 + tok] = b;
            }
        }
        __syncthreads();
        if (w < 2) {
            float t = 0.f;
#pragma unroll
            for (int c = 0; c < 10; ++c) t += red[w * 640 + c * 64 + tok];
            unsigned long long g = ((unsigned long long)GTAG << 32) | (unsigned long long)__float_as_uint(t);
            __hip_atomic_store((gu64*)(gran + ((rt * 8 + ct) * 2 + w) * 64 + tok), g, __ATOMIC_RELAXED, __HIP_MEMORY_SCOPE_AGENT);
        }
        {
            unsigned long long g0, g1;
            gu64* p0 = (gu64*)(gran + ((rt * 8 + w) * 2 + 0) * 64 + tok);
            gu64* p1 = (gu64*)(gran + ((rt * 8 + w) * 2 + 1) * 64 + tok);
            for (unsigned spins = 0; spins < (1u << 22); ++spins) {
                g0 = __hip_atomic_load(p0, __ATOMIC_RELAXED, __HIP_MEMORY_SCOPE_AGENT);
                g1 = __hip_atomic_load(p1, __ATOMIC_RELAXED, __HIP_MEMORY_SCOPE_AGENT);
                bool ok = ((u32)(g0 >> 32) == GTAG) && ((u32)(g1 >> 32) == GTAG);
                if (__all(ok)) break;
                __builtin_amdgcn_s_sleep(1);
            }
            red2[w * 64 + tok] = __uint_as_float((u32)g0);
            red2[512 + w * 64 + tok] = __uint_as_float((u32)g1);
        }
        __syncthreads();
        float a = 0.f, b = 0.f;
#pragma unroll
        for (int c = 0; c < 8; ++c) { a += red2[c * 64 + tok]; b += red2[512 + c * 64 + tok]; }
        const float mean = a * (1.0f / 640.0f);
        float var = b * (1.0f / 640.0f) - mean * mean;
        var = var > 0.f ? var : 0.f;
        float mb, rinv;
        ln_finish(mean, var, mb, rinv);
#pragma unroll
        for (int it = 0; it < 2; ++it) {
            int cc = w + 8 * it;
            if (cc < 10) {
                u32x4 o;
#pragma unroll
                for (int e = 0; e < 4; ++e) {
                    float xa = lo2f(x2[it][e]), xb = hi2f(x2[it][e]);
                    float qa = rbf(rbf(xa - mb) * rinv), qb = rbf(rbf(xb - mb) * rinv);
                    float ga = rbf(qa * lo2f(yv[it][e])), gb = rbf(qb * hi2f(yv[it][e]));
                    o[e] = pack2(xa + ga, xb + gb);
                }
                *(u32x4*)(xout + xf_idx(token, 80 * ct + 8 * cc)) = o;
            }
        }
    }
}

extern "C" void kernel_launch(void* const* d_in, const int* in_sizes, int n_in,
                              void* d_out, int out_size, void* d_ws, size_t ws_size,
                              hipStream_t stream) {
    const u16* q = (const u16*)d_in[0];
    const u16* k = (const u16*)d_in[1];
    const u16* v = (const u16*)d_in[2];
    const u16* sk = (const u16*)d_in[3];
    const u16* ssmk = (const u16*)d_in[4];
    const u16* ssmD = (const u16*)d_in[5];
    const u16* wg = (const u16*)d_in[7];
    const u16* wu = (const u16*)d_in[8];
    const u16* wd = (const u16*)d_in[9];
    u16* out = (u16*)d_out;

    char* ws = (char*)d_ws;
    constexpr size_t SZ_X = (size_t)S_ * D_ * 2;
    size_t off = 0;
    u16* W1p = (u16*)(ws + off); off += (size_t)NUNIT * KS1 * 1024;
    u16* kvR = (u16*)(ws + off); off += SZ_X;
    u16* yF = (u16*)(ws + off); off += SZ_X;
    u16* xaF = (u16*)(ws + off); off += SZ_X;
    u16* xbF = (u16*)(ws + off); off += SZ_X;
    u16* qF = (u16*)(ws + off); off += SZ_X;
    u16* W2p = (u16*)(ws + off); off += (size_t)40 * KS2 * 1024;
    u16* hp = (u16*)(ws + off); off += (size_t)128 * KS2 * 1024;
    f32x2* qs = (f32x2*)(ws + off); off += (size_t)S_ * 8;
    float* cs = (float*)(ws + off); off += (size_t)NUNIT * 32 * 4;
    unsigned long long* gran = (unsigned long long*)(ws + off); off += (size_t)GRAN_WORDS * 8;

    prep_kernel<<<320 + 512, 256, 0, stream>>>(k, v, sk, kvR, q, qF, qs, (u32x4*)gran);
    conv_kernel<<<NCONV + NREP1 + NREP2 + NCS, 256, 0, stream>>>(kvR, ssmk, ssmD, yF, wg, wu, wd, W1p, W2p, qF, qs, xaF, cs);
    gemm1_kernel<<<256, 512, 0, stream>>>(xaF, W1p, cs, hp);
    gemm2_kernel<true><<<256, 512, 0, stream>>>(hp, W2p, xaF, yF, xbF, gran);
    gemm1_kernel<<<256, 512, 0, stream>>>(xbF, W1p, cs, hp);
    gemm2_kernel<false><<<256, 512, 0, stream>>>(hp, W2p, xbF, yF, out, gran);
}
```

```cpp
#include <hip/hip_runtime.h>
#include <hip/hip_bf16.h>
#include <stdint.h>

typedef unsigned short u16;
typedef unsigned int u32;
typedef __attribute__((ext_vector_type(8))) short bf16x8;
typedef __attribute__((ext_vector_type(4))) float f32x4;
typedef __attribute__((ext_vector_type(16))) float f32x16;
typedef __attribute__((ext_vector_type(4))) u32 u32x4;
typedef __attribute__((ext_vector_type(2))) u32 u32x2;
typedef __attribute__((ext_vector_type(2))) float f32x2;
typedef __attribute__((ext_vector_type(2))) __bf16 bf16x2v;

constexpr int S_ = 2048, D_ = 640, F_ = 1728;
constexpr int NUNIT = 108;
constexpr int KS1 = 40;
constexpr int KS2 = 54;

__device__ __forceinline__ float lo2f(u32 v) { return __uint_as_float(v << 16); }
__device__ __forceinline__ float hi2f(u32 v) { return __uint_as_float(v & 0xffff0000u); }
__device__ __forceinline__ u32 pack2(float a, float b) {
    f32x2 f = {a, b};
    bf16x2v r = __builtin_convertvector(f, bf16x2v);
    return __builtin_bit_cast(u32, r);
}
__device__ __forceinline__ float rbf(float a) {
    __bf16 b = (__bf16)a;
    return __uint_as_float(((u32)__builtin_bit_cast(u16, b)) << 16);
}
__device__ __forceinline__ u16 f2bf(float a) {
    __bf16 b = (__bf16)a;
    return __builtin_bit_cast(u16, b);
}

__device__ __forceinline__ float rowsum16(float x) {
    x += __builtin_bit_cast(float, __builtin_amdgcn_update_dpp(0, __builtin_bit_cast(int, x), 0x128, 0xf, 0xf, false));
    x += __builtin_bit_cast(float, __builtin_amdgcn_update_dpp(0, __builtin_bit_cast(int, x), 0x124, 0xf, 0xf, false));
    x += __builtin_bit_cast(float, __builtin_amdgcn_update_dpp(0, __builtin_bit_cast(int, x), 0x122, 0xf, 0xf, false));
    x += __builtin_bit_cast(float, __builtin_amdgcn_update_dpp(0, __builtin_bit_cast(int, x), 0x121, 0xf, 0xf, false));
    return x;
}

__device__ __forceinline__ float wavesum(float x) {
    x = rowsum16(x);
    x += __shfl_xor(x, 16);
    x += __shfl_xor(x, 32);
    return x;
}
__device__ __forceinline__ long xf_idx(int token, int d) {
    return ((long)((token >> 5) * 80 + (d >> 3)) * 32 + (token & 31)) * 8 + (d & 7);
}
__device__ __forceinline__ float dot2bf(u32 a, u32 b, float c) {
    return __builtin_amdgcn_fdot2_f32_bf16(__builtin_bit_cast(bf16x2v, a), __builtin_bit_cast(bf16x2v, b), c, false);
}
__device__ __forceinline__ void ln_finish(float mean, float var, float& mb, float& rinv) {
    mb = rbf(mean);
    float den = rbf(__builtin_sqrtf(rbf(rbf(var) + 1e-5f)));
    rinv = 1.0f / den;
}
constexpr u32 GTAG = 0x13572468u;
constexpr int GRAN_WORDS = 32 * 8 * 2 * 64;

__global__ __launch_bounds__(256) void prep_kernel(const u16* __restrict__ k, const u16* __restrict__ v,
                                                   const u16* __restrict__ sk, u16* __restrict__ kvR,
                                                   const u16* __restrict__ q, u16* __restrict__ qF,
                                                   f32x2* __restrict__ qs, u32x4* __restrict__ gran) {
    __shared__ __attribute__((aligned(16))) u32 T[64 * 32];
    if (blockIdx.x >= 320) {
        const int b = blockIdx.x - 320;
        if (b < 64) { u32x4 z = {0u, 0u, 0u, 0u}; gran[b * 256 + threadIdx.x] = z; }
        const int lane = threadIdx.x & 63;
        const int row = b * 4 + (threadIdx.x >> 6);
        const u32* xs = (const u32*)(q + (long)row * D_);
        u32 xv[5];
#pragma unroll
        for (int m = 0; m < 5; ++m) xv[m] = xs[lane + 64 * m];
        float sum = 0.f;
#pragma unroll
        for (int m = 0; m < 5; ++m) sum += lo2f(xv[m]) + hi2f(xv[m]);
        sum = wavesum(sum);
        float mean = sum * (1.0f / 640.0f);
        float sq = 0.f;
#pragma unroll
        for (int m = 0; m < 5; ++m) {
            float a = lo2f(xv[m]) - mean, c = hi2f(xv[m]) - mean;
            sq += a * a + c * c;
        }
        sq = wavesum(sq);
        float mb, rinv;
        ln_finish(mean, sq * (1.0f / 640.0f), mb, rinv);
        if (lane == 0) { f32x2 o = {mb, rinv}; qs[row] = o; }
#pragma unroll
        for (int m = 0; m < 5; ++m) *(u32*)(qF + xf_idx(row, 2 * (lane + 64 * m))) = xv[m];
        return;
    }
    const int st = blockIdx.x / 10, dt = blockIdx.x % 10;
    const int s0 = st * 64, d0 = dt * 64;
    const int t = threadIdx.x, dc = t & 7, rp = t >> 3;
    const int s = s0 + 2 * rp, d = d0 + 8 * dc;
    u32x4 kr[5];
#pragma unroll
    for (int i = 0; i < 5; ++i) {
        int row = s - 3 + i;
        u32x4 z = {0u, 0u, 0u, 0u};
        kr[i] = (row >= 0) ? *(const u32x4*)(k + (long)row * D_ + d) : z;
    }
    u32x4 v0 = *(const u32x4*)(v + (long)s * D_ + d);
    u32x4 v1 = *(const u32x4*)(v + (long)(s + 1) * D_ + d);
    u32x4 skv[4];
#pragma unroll
    for (int i = 0; i < 4; ++i) skv[i] = *(const u32x4*)(sk + (long)d * 4 + 8 * i);
    const int p = 31 - rp;
#pragma unroll
    for (int e = 0; e < 8; ++e) {
        u32 w01 = skv[e >> 1][(e & 1) * 2 + 0], w23 = skv[e >> 1][(e & 1) * 2 + 1];
        float c0 = lo2f(w01), c1 = hi2f(w01), c2 = lo2f(w23), c3 = hi2f(w23);
        float kk[5];
#pragma unroll
        for (int i = 0; i < 5; ++i) {
            u32 wv = kr[i][e >> 1];
            kk[i] = (e & 1) ? hi2f(wv) : lo2f(wv);
        }
        float va = (e & 1) ? hi2f(v0[e >> 1]) : lo2f(v0[e >> 1]);
        float vb = (e & 1) ? hi2f(v1[e >> 1]) : lo2f(v1[e >> 1]);
        float a = rbf(kk[0] * c3);
        a = rbf(a + rbf(kk[1] * c2));
        a = rbf(a + rbf(kk[2] * c1));
        a = rbf(a + rbf(kk[3] * c0));
        float b = rbf(kk[1] * c3);
        b = rbf(b + rbf(kk[2] * c2));
        b = rbf(b + rbf(kk[3] * c1));
        b = rbf(b + rbf(kk[4] * c0));
        u32 pk = pack2(b * vb, a * va);
        int dl = 8 * dc + e;
        T[dl * 32 + ((((p >> 2) ^ dc)) << 2) + (p & 3)] = pk;
    }
    __syncthreads();
#pragma unroll
    for (int it = 0; it < 2; ++it) {
        int idx = t + 256 * it;
        int dl = idx >> 3, c = idx & 7;
        u32x4 o = *(const u32x4*)&T[dl * 32 + ((c ^ (dl >> 3)) << 2)];
        *(u32x4*)(kvR + (long)(d0 + dl) * S_ + (1984 - s0) + 8 * c) = o;
    }
}

constexpr int HC_STRIDE = 4416;
constexpr int KV_BYTES = 47 * 144;
constexpr int WAVE_LDS = 4 * HC_STRIDE + KV_BYTES;
constexpr int YBUF_OFF = 4 * WAVE_LDS;
constexpr int CONV_LDS = YBUF_OFF + 2048 * 8;
constexpr int NCONV = 160;
constexpr int NREP1 = 160;
constexpr int NREP2 = 108;
constexpr int NCS = 54;

__global__ __launch_bounds__(256, 1) void conv_kernel(const u16* __restrict__ kvR, const u16* __restrict__ ssmk,
                                                      const u16* __restrict__ ssmD, u16* __restrict__ yF,
                                                      const u16* __restrict__ wg, const u16* __restrict__ wu,
                                                      const u16* __restrict__ wd, u16* __restrict__ W1p,
                                                      u16* __restrict__ W2p, const u16* __restrict__ qF,
                                                      const f32x2* __restrict__ qs, u16* __restrict__ xaF,
                                                      float* __restrict__ cs) {
    __shared__ __attribute__((aligned(16))) char lds[CONV_LDS];
    const int tid = threadIdx.x, lane = tid & 63, w = tid >> 6;
    const int bid = blockIdx.x;
    if (bid < NCONV) {
        const int ch = bid * 4 + w;
        char* wl = lds + w * WAVE_LDS;
        char* kvl = wl + 4 * HC_STRIDE;
        const u32x4* hsrc = (const u32x4*)(ssmk + (long)ch * S_);
        const u32x4* ksrc = (const u32x4*)(kvR + (long)ch * S_);
        u32x4 kvv[4];
#pragma unroll
        for (int m = 0; m < 4; ++m) kvv[m] = ksrc[lane + 64 * m];
#pragma unroll
        for (int m = 0; m < 5; ++m) {
            int c = lane - 8 + 64 * m;
            if (c < 256) {
                u32x4 z = {0u, 0u, 0u, 0u};
                u32x4 a = (c >= 0) ? hsrc[c] : z;
                u32x4 b = (c + 1 >= 0 && c + 1 < 256) ? hsrc[c + 1] : z;
                u32 ww[8] = {a[0], a[1], a[2], a[3], b[0], b[1], b[2], b[3]};
                u32x4 o0, o1, o2, o3;
#pragma unroll
                for (int i = 0; i < 4; ++i) {
                    o0[i] = __builtin_amdgcn_alignbit(ww[i + 1], ww[i], 16);
                    o1[i] = ww[i + 1];
                    o2[i] = __builtin_amdgcn_alignbit(ww[i + 2], ww[i + 1], 16);
                    o3[i] = ww[i + 2];
                }
                char* dst = wl + (c + 8) * 16;
                *(u32x4*)(dst + 0 * HC_STRIDE) = o0;
                *(u32x4*)(dst + 1 * HC_STRIDE) = o1;
                *(u32x4*)(dst + 2 * HC_STRIDE) = o2;
                *(u32x4*)(dst + 3 * HC_STRIDE) = o3;
            }
        }
#pragma unroll
        for (int m = 0; m < 4; ++m) {
            int c = lane + 64 * m;
            *(u32x4*)(kvl + (c >> 3) * 144 + (c & 7) * 16) = kvv[m];
        }
        {
            u32x4 z = {0u, 0u, 0u, 0u};
#pragma unroll
            for (int m = 0; m < 3; ++m) {
                int c = lane + 64 * m;
                if (c < 135) *(u32x4*)(kvl + 32 * 144 + c * 16) = z;
            }
        }
        __builtin_amdgcn_s_waitcnt(0xC07F);
        __builtin_amdgcn_wave_barrier();
        u32x2 qv[8];
        f32x2 qst[8];
#pragma unroll
        for (int it = 0; it < 8; ++it) {
            int s = tid + 256 * it;
            qv[it] = *(const u32x2*)(qF + xf_idx(s, bid * 4));
            qst[it] = qs[s];
        }

        const int r = lane & 15, kq = lane >> 4;
        u32 aoffA = (u32)(w * WAVE_LDS + (r & 3) * HC_STRIDE + 2 * ((r & 12) + 8 * kq));
        u32 aoffB = aoffA + 8, aoffC = aoffA + 32, aoffD = aoffA + 40;
        asm volatile("" : "+v"(aoffB));
        asm volatile("" : "+v"(aoffC));
        asm volatile("" : "+v"(aoffD));
        const u32 boff = (u32)(w * WAVE_LDS + 4 * HC_STRIDE + 16 * (9 * (31 - r) + kq));

        f32x4 acc[2][4];
#pragma unroll
        for (int n = 0; n < 2; ++n)
#pragma unroll
            for (int m = 0; m < 4; ++m) acc[n][m] = (f32x4){0.f, 0.f, 0.f, 0.f};

        bf16x8 A[136], B0[32], B1[64];
#define LOADA(i) do { \
            u32x2 l0 = *(const u32x2*)(lds + (((i) & 1) ? aoffC : aoffA) + 64 * ((i) >> 1)); \
            u32x2 l1 = *(const u32x2*)(lds + (((i) & 1) ? aoffD : aoffB) + 64 * ((i) >> 1)); \
            u32x4 t4 = {l0[0], l0[1], l1[0], l1[1]}; \
            A[i] = __builtin_bit_cast(bf16x8, t4); } while (0)
#define KSO(ks) (16 * (9 * ((ks) >> 1) + 4 * ((ks) & 1)))
#define LOADB(ks) do { \
            B1[ks] = *(const bf16x8*)(lds + boff + KSO(ks) - 16 * 9 * 16); \
            if ((ks) < 32) B0[(ks) < 32 ? (ks) : 0] = *(const bf16x8*)(lds + boff + KSO(ks)); } while (0)
#pragma unroll
        for (int i = 0; i < 6; ++i) LOADA(i);
        LOADB(0);
        LOADB(1);
        __builtin_amdgcn_sched_barrier(0);
#pragma unroll
        for (int ks = 0; ks < 64; ++ks) {
            if (ks + 2 < 64) {
                LOADA(2 * ks + 6);
                LOADA(2 * ks + 7);
                LOADB(ks + 2);
            }
            __builtin_amdgcn_sched_barrier(0);
#pragma unroll
            for (int m = 0; m < 4; ++m)
                acc[1][m] = __builtin_amdgcn_mfma_f32_16x16x32_bf16(A[2 * ks + m], B1[ks], acc[1][m], 0, 0, 0);
            if (ks < 32) {
#pragma unroll
                for (int m = 0; m < 4; ++m)
                    acc[0][m] = __builtin_amdgcn_mfma_f32_16x16x32_bf16(A[2 * ks + m], B0[ks < 32 ? ks : 0], acc[0][m], 0, 0, 0);
            }
            __builtin_amdgcn_sched_barrier(0);
        }
#undef LOADA
#undef LOADB
#undef KSO
        const float Dv = lo2f((u32)ssmD[ch]);
        u16* ybuf = (u16*)(lds + YBUF_OFF);
#pragma unroll
        for (int n = 0; n < 2; ++n)
#pragma unroll
            for (int m = 0; m < 4; ++m) {
                int a = 16 * n + r;
                int ib = 60 - 16 * m - 4 * kq;
                u32x2 kk = *(const u32x2*)(kvl + (31 - a) * 144 + ib * 2);
                float kvf[4] = {hi2f(kk[1]), lo2f(kk[1]), hi2f(kk[0]), lo2f(kk[0])};
                int sb = 64 * a + 16 * m + 4 * kq;
#pragma unroll
                for (int j = 0; j < 4; ++j) {
                    float yc = rbf(acc[n][m][j]);
                    float tt = rbf(kvf[j] * Dv);
                    ybuf[(sb + j) * 4 + w] = f2bf(yc + tt);
                }
            }
        __syncthreads();
#pragma unroll
        for (int it = 0; it < 8; ++it) {
            int s = tid + 256 * it;
            u32x2 yo = *(const u32x2*)(ybuf + s * 4);
            float mb = qst[it][0], rinv = qst[it][1];
            u32x2 xo;
#pragma unroll
            for (int j = 0; j < 2; ++j) {
                float a = lo2f(qv[it][j]), b = hi2f(qv[it][j]);
                float qa = rbf(rbf(a - mb) * rinv), qb = rbf(rbf(b - mb) * rinv);
                float ga = rbf(qa * lo2f(yo[j])), gb = rbf(qb * hi2f(yo[j]));
                xo[j] = pack2(a + ga, b + gb);
            }
            long idx = xf_idx(s, bid * 4);
            *(u32x2*)(yF + idx) = yo;
            *(u32x2*)(xaF + idx) = xo;
        }
    } else if (bid < NCONV + NREP1) {
        const int b = bid - NCONV;
        const int ks = b >> 2, ug = b & 3;
        u16* T = (u16*)lds;
        constexpr int TS = 440;
        for (int i = tid; i < 1728; i += 256) {
            int mat = i / 864, rem = i % 864, kr = rem / 54, cc = rem % 54;
            const u16* src = (mat ? wu : wg) + (long)(16 * ks + kr) * F_ + 432 * ug + 8 * cc;
            *(u32x4*)(T + (mat * 16 + kr) * TS + 8 * cc) = *(const u32x4*)src;
        }
        __syncthreads();
        for (int i = tid; i < 1728; i += 256) {
            int uu = i >> 6, l = i & 63;
            int ii = l & 31, hh = l >> 5;
            int mat = ii >> 4, col = 16 * uu + (ii & 15);
            const u16* tp = T + (mat * 16 + 8 * hh) * TS + col;
            u32x4 o;
#pragma unroll
            for (int j = 0; j < 4; ++j) o[j] = (u32)tp[(2 * j) * TS] | ((u32)tp[(2 * j + 1) * TS] << 16);
            *(u32x4*)(W1p + ((long)((27 * ug + uu) * KS1 + ks) * 64 + l) * 8) = o;
        }
    } else if (bid >= NCONV + NREP1 + NREP2) {
        const int b = bid - NCONV - NREP1 - NREP2;
        const int mat = b / 27, cb = b % 27;
        const u16* wsrc = mat ? wu : wg;
        const int cp = tid & 31, kg = tid >> 5;
        float a0 = 0.f, a1 = 0.f;
#pragma unroll 16
        for (int i = 0; i < 80; ++i) {
            u32 wv = *(const u32*)(wsrc + (long)(kg * 80 + i) * F_ + cb * 64 + 2 * cp);
            a0 += lo2f(wv);
            a1 += hi2f(wv);
        }
        float* T = (float*)lds;
        T[kg * 64 + 2 * cp] = a0;
        T[kg * 64 + 2 * cp + 1] = a1;
        __syncthreads();
        if (tid < 64) {
            float t = 0.f;
#pragma unroll
            for (int g = 0; g < 8; ++g) t += T[g * 64 + tid];
            int f = cb * 64 + tid;
            cs[(f >> 4) * 32 + mat * 16 + (f & 15)] = t;
        }
    } else {
        const int b = bid - NCONV - NREP1;
        const int s = b >> 1, dh = b & 1;
        u16* T = (u16*)lds;
        constexpr int TS = 328;
        for (int i = tid; i < 1280; i += 256) {
            int fr = i / 40, cc = i % 40;
            *(u32x4*)(T + fr * TS + 8 * cc) = *(const u32x4*)(wd + (long)(32 * s + fr) * D_ + 320 * dh + 8 * cc);
        }
        __syncthreads();
        for (int i = tid; i < 1280; i += 256) {
            int ntl = i >> 6, l = i & 63;
            int idx = l & 15, kq = l >> 4;
            int fb = 16 * (kq >> 1) + 4 * (kq & 1);
            const u16* tp = T + 16 * ntl + idx;
            u32x4 o;
            o[0] = (u32)tp[(fb + 0) * TS] | ((u32)tp[(fb + 1) * TS] << 16);
            o[1] = (u32)tp[(fb + 2) * TS] | ((u32)tp[(fb + 3) * TS] << 16);
            o[2] = (u32)tp[(fb + 8) * TS] | ((u32)tp[(fb + 9) * TS] << 16);
            o[3] = (u32)tp[(fb + 10) * TS] | ((u32)tp[(fb + 11) * TS] << 16);
            *(u32x4*)(W2p + ((long)((20 * dh + ntl) * KS2 + s) * 64 + l) * 8) = o;
        }
    }
}

constexpr int G1_STAGE = 44 * 1024;
constexpr int G1_CS_OFF = 3 * G1_STAGE;

__device__ __forceinline__ void g1_epi_unit(const f32x16& a, const char* cp, float mb, float rinv, u16* hp,
                                            int token, int u, int hh) {
    f32x4 c0 = *(const f32x4*)(cp), c1 = *(const f32x4*)(cp + 32);
    f32x4 c2 = *(const f32x4*)(cp + 64), c3 = *(const f32x4*)(cp + 96);
    float hv[8];
#pragma unroll
    for (int e = 0; e < 8; ++e) {
        float cg = (e < 4) ? c0[e & 3] : c1[e & 3];
        float cu = (e < 4) ? c2[e & 3] : c3[e & 3];
        u32 gu = pack2((a[e] - mb * cg) * rinv, (a[e + 8] - mb * cu) * rinv);
        float g = lo2f(gu), uu = hi2f(gu);
        float sg = rbf(__builtin_amdgcn_rcpf(1.0f + __expf(-g)));
        hv[e] = sg * uu;
    }
    u32x4 o = {pack2(hv[0], hv[1]), pack2(hv[2], hv[3]), pack2(hv[4], hv[5]), pack2(hv[6], hv[7])};
    long idx = ((long)((token >> 4) * KS2 + (u >> 1)) * 64 + (token & 15) + 16 * (2 * (u & 1) + hh));
    *(u32x4*)(hp + idx * 8) = o;
}

constexpr int G1_ST_OFF = G1_CS_OFF + 1024;

__global__ __launch_bounds__(512, 2) void gemm1_kernel(const u16* __restrict__ xF, const u16* __restrict__ W1p,
                                                       const float* __restrict__ cs, u16* __restrict__ hp) {
    __shared__ __attribute__((aligned(16))) char lds[3 * G1_STAGE + 2048];
    const int tid = threadIdx.x, lane = tid & 63;
    const int w = __builtin_amdgcn_readfirstlane(tid >> 6);
    const int xcd = blockIdx.x & 7, jj = blockIdx.x >> 3;
    const int rt = 8 * (xcd >> 2) + (jj & 7), ct = 4 * (xcd & 3) + (jj >> 3);
    const int ubase = ct * 7;
    const int tb = w & 3;
    const int tk = lane & 31, hh = lane >> 5;
    const int token = 128 * rt + 32 * tb + tk;
    u32 lb[3];
#pragma unroll
    for (int b = 0; b < 3; ++b) { lb[b] = (u32)(b * G1_STAGE + lane * 16); asm volatile("" : "+v"(lb[b])); }

    if (w >= 4) {
        const int lw = w - 4;
        const char* base = (const char*)W1p;
        const u32 xoff = (u32)((const char*)xF - base);
        u32 src[11];
#pragma unroll
        for (int i = 0; i < 11; ++i) {
            int p = lw * 11 + i;
            int stream = p >> 2, ksl = p & 3;
            int uu = ubase + stream - 4;
            if (uu > NUNIT - 1) uu = NUNIT - 1;
            src[i] = (stream < 4) ? xoff + (u32)((((4 * rt + stream) * KS1 + ksl) * 64 + lane) * 16)
                                  : (u32)(((uu * KS1 + ksl) * 64 + lane) * 16);
        }
#define G1_ISSUE(s) do { \
        _Pragma("unroll") for (int i = 0; i < 11; ++i) \
            __builtin_amdgcn_global_load_lds((const u32*)(base + (src[i] + (u32)((s) * 4096))), \
                (u32*)(lds + ((s) % 3) * G1_STAGE + (lw * 11 + i) * 1024 + lane * 16), 16, 0, 0); \
        } while (0)
        G1_ISSUE(0);
        G1_ISSUE(1);
        float s1 = 0.f, s2 = 0.f;
#pragma unroll
        for (int s = 0; s < 10; ++s) {
            if (s < 9) asm volatile("s_waitcnt vmcnt(11) lgkmcnt(0)" ::: "memory");
            else asm volatile("s_waitcnt vmcnt(0) lgkmcnt(0)" ::: "memory");
            __builtin_amdgcn_s_barrier();
            asm volatile("" ::: "memory");
            u32x4 xx[4];
#pragma unroll
            for (int j = 0; j < 4; ++j) xx[j] = *(const u32x4*)(lds + lb[s % 3] + tb * 4096 + j * 1024);
            if (s + 2 < 10) G1_ISSUE(s + 2);
#pragma unroll
            for (int j = 0; j < 4; ++j)
#pragma unroll
                for (int e = 0; e < 4; ++e) {
                    s1 = dot2bf(xx[j][e], 0x3f803f80u, s1);
                    s2 = dot2bf(xx[j][e], xx[j][e], s2);
                }
        }
#undef G1_ISSUE
        s1 += __shfl_xor(s1, 32);
        s2 += __shfl_xor(s2, 32);
        const float mean = s1 * (1.0f / 640.0f);
        float var = s2 * (1.0f / 640.0f) - mean * mean;
        var = var > 0.f ? var : 0.f;
        float mb, rinv;
        ln_finish(mean, var, mb, rinv);
        if (lane < 32) { f32x2 st = {mb, rinv}; *(f32x2*)(lds + G1_ST_OFF + (tb * 32 + tk) * 8) = st; }
        __syncthreads();
#pragma unroll
        for (int i = 4; i < 7; ++i) {
            int u = ubase + i;
            if (u < NUNIT) {
                f32x16 a;
#pragma unroll
                for (int r = 0; r < 4; ++r) {
                    f32x4 t = *(const f32x4*)(lds + G1_STAGE + (tb * 12 + (i - 4) * 4 + r) * 1024 + lane * 16);
                    a[4 * r] = t[0]; a[4 * r + 1] = t[1]; a[4 * r + 2] = t[2]; a[4 * r + 3] = t[3];
                }
                g1_epi_unit(a, lds + G1_CS_OFF + i * 128 + hh * 16, mb, rinv, hp, token, u, hh);
            }
        }
        return;
    }
    if (lane < 56) {
        int e = ubase * 32 + w * 56 + lane;
        float csv = (e < NUNIT * 32) ? cs[e] : 0.f;
        *(float*)(lds + G1_CS_OFF + (w * 56 + lane) * 4) = csv;
    }
    f32x16 acc[7];
#pragma unroll
    for (int a = 0; a < 7; ++a)
#pragma unroll
        for (int i = 0; i < 16; ++i) acc[a][i] = 0.f;
    bf16x8 xf[3], wf[3][7];
#pragma unroll
    for (int g = 0; g < 42; ++g) {
        const int s = g >> 2, j = g & 3;
        if (g < 40) {
            if (j == 0) {
                asm volatile("s_waitcnt lgkmcnt(0)" ::: "memory");
                __builtin_amdgcn_s_barrier();
                asm volatile("" ::: "memory");
            }
            xf[g % 3] = *(const bf16x8*)(lds + lb[s % 3] + tb * 4096 + j * 1024);
#pragma unroll
            for (int i = 0; i < 7; ++i) wf[g % 3][i] = *(const bf16x8*)(lds + lb[s % 3] + (4 + i) * 4096 + j * 1024);
        }
        if (g >= 2) {
            const int c = (g - 2) % 3;
#pragma unroll
            for (int i = 0; i < 7; ++i)
                acc[i] = __builtin_amdgcn_mfma_f32_32x32x16_bf16(wf[c][i], xf[c], acc[i], 0, 0, 0);
#pragma unroll
            for (int i = 0; i < 7; ++i) {
                __builtin_amdgcn_sched_group_barrier(0x8, 1, 0);
                __builtin_amdgcn_sched_group_barrier(0x100, 1, 0);
            }
        }
        __builtin_amdgcn_sched_barrier(0);
    }
#pragma unroll
    for (int i = 4; i < 7; ++i)
#pragma unroll
        for (int r = 0; r < 4; ++r) {
            f32x4 t = {acc[i][4 * r], acc[i][4 * r + 1], acc[i][4 * r + 2], acc[i][4 * r + 3]};
            *(f32x4*)(lds + G1_STAGE + (tb * 12 + (i - 4) * 4 + r) * 1024 + lane * 16) = t;
        }
    __syncthreads();
    f32x2 st = *(const f32x2*)(lds + G1_ST_OFF + (tb * 32 + tk) * 8);
#pragma unroll
    for (int i = 0; i < 4; ++i) {
        int u = ubase + i;
        if (u < NUNIT) g1_epi_unit(acc[i], lds + G1_CS_OFF + i * 128 + hh * 16, st[0], st[1], hp, token, u, hh);
    }
}

constexpr int PROW = 336;
constexpr int PTILE = 64 * PROW;
constexpr int PD2 = 3;
typedef __attribute__((address_space(1))) unsigned long long gu64;

template <bool MID>
__global__ __launch_bounds__(512, 2) void gemm2_kernel(const u16* __restrict__ hp, const u16* __restrict__ W2p,
                                                       const u16* __restrict__ xresF, const u16* __restrict__ yF,
                                                       u16* __restrict__ xout, unsigned long long* gran) {
    __shared__ __attribute__((aligned(16))) char lds[4 * PTILE];
    const int tid = threadIdx.x, lane = tid & 63, w = tid >> 6;
    const int xcd = blockIdx.x & 7, jj = blockIdx.x >> 3;
    const int rt = 8 * (xcd >> 1) + (jj & 7), ct = 4 * (xcd & 1) + (jj >> 3);

    const bf16x8* hb = (const bf16x8*)hp + (long)(4 * rt) * KS2 * 64 + lane;
    const bf16x8* wbp = (const bf16x8*)W2p + (long)(5 * ct) * KS2 * 64 + lane;
    const int nsteps = (w < 6) ? 7 : 6;

    bf16x8 hf[PD2][4], wf[PD2][5];
#pragma unroll
    for (int i = 0; i < PD2; ++i) {
        int s = w + 8 * i;
#pragma unroll
        for (int m = 0; m < 4; ++m) hf[i][m] = hb[(long)(m * KS2 + s) * 64];
#pragma unroll
        for (int n = 0; n < 5; ++n) wf[i][n] = wbp[(long)(n * KS2 + s) * 64];
    }
    const int tok = lane;
    const int token = 64 * rt + tok;
    u32x4 res[2], yv[2];
#pragma unroll
    for (int it = 0; it < 2; ++it) {
        int cc = w + 8 * it;
        if (cc < 10) {
            long idx = xf_idx(token, 80 * ct + 8 * cc);
            res[it] = *(const u32x4*)(xresF + idx);
            if (MID) yv[it] = *(const u32x4*)(yF + idx);
        }
    }
    f32x4 acc[5][4];
#pragma unroll
    for (int n = 0; n < 5; ++n)
#pragma unroll
        for (int m = 0; m < 4; ++m) acc[n][m] = (f32x4){0.f, 0.f, 0.f, 0.f};
#pragma unroll
    for (int i = 0; i < 7; ++i) {
        if (i < nsteps) {
#pragma unroll
            for (int n = 0; n < 5; ++n)
#pragma unroll
                for (int m = 0; m < 4; ++m)
                    acc[n][m] = __builtin_amdgcn_mfma_f32_16x16x32_bf16(wf[i % PD2][n], hf[i % PD2][m], acc[n][m], 0, 0, 0);
            if (i + PD2 < nsteps) {
                int s = w + 8 * (i + PD2);
#pragma unroll
                for (int m = 0; m < 4; ++m) hf[i % PD2][m] = hb[(long)(m * KS2 + s) * 64];
#pragma unroll
                for (int n = 0; n < 5; ++n) wf[i % PD2][n] = wbp[(long)(n * KS2 + s) * 64];
            }
        }
    }
    const int tl = lane & 15, q = lane >> 4;
    char* slot = lds + (w & 3) * PTILE + tl * PROW + q * 16;
    if (w >= 4) {
#pragma unroll
        for (int n = 0; n < 5; ++n)
#pragma unroll
            for (int m = 0; m < 4; ++m) *(f32x4*)(slot + m * 16 * PROW + n * 64) = acc[n][m];
    }
    __syncthreads();
    if (w < 4) {
#pragma unroll
        for (int n = 0; n < 5; ++n)
#pragma unroll
            for (int m = 0; m < 4; ++m) {
                f32x4 o = *(const f32x4*)(slot + m * 16 * PROW + n * 64);
                *(f32x4*)(slot + m * 16 * PROW + n * 64) = o + acc[n][m];
            }
    }
    __syncthreads();
    u32x4 x2[2];
#pragma unroll
    for (int it = 0; it < 2; ++it) {
        int cc = w + 8 * it;
        if (cc < 10) {
            const char* p = lds + tok * PROW + cc * 32;
            f32x4 s0 = *(const f32x4*)(p), s1 = *(const f32x4*)(p + 16);
#pragma unroll
            for (int k = 1; k < 4; ++k) {
                s0 += *(const f32x4*)(p + k * PTILE);
                s1 += *(const f32x4*)(p + k * PTILE + 16);
            }
            u32x4 rs = res[it];
            x2[it][0] = pack2(lo2f(rs[0]) + rbf(s0[0]), hi2f(rs[0]) + rbf(s0[1]));
            x2[it][1] = pack2(lo2f(rs[1]) + rbf(s0[2]), hi2f(rs[1]) + rbf(s0[3]));
            x2[it][2] = pack2(lo2f(rs[2]) + rbf(s1[0]), hi2f(rs[2]) + rbf(s1[1]));
            x2[it][3] = pack2(lo2f(rs[3]) + rbf(s1[2]), hi2f(rs[3]) + rbf(s1[3]));
            if (!MID) *(u32x4*)(xout + (long)token * D_ + 80 * ct + 8 * cc) = x2[it];
        }
    }
    if (MID) {
        __syncthreads();
        float* red = (float*)lds;
        float* red2 = (float*)(lds + 8192);
#pragma unroll
        for (int it = 0; it < 2; ++it) {
            int cc = w + 8 * it;
            if (cc < 10) {
                float a = 0.f, b = 0.f;
#pragma unroll
                for (int e = 0; e < 4; ++e) { a = dot2bf(x2[it][e], 0x3f803f80u, a); b = dot2bf(x2[it][e], x2[it][e], b); }
                red[cc * 64 + tok] = a;
                red[640 + cc * 64 + tok] = b;
            }
        }
        __syncthreads();
        if (w < 2) {
            float t = 0.f;
#pragma unroll
            for (int c = 0; c < 10; ++c) t += red[w * 640 + c * 64 + tok];
            unsigned long long g = ((unsigned long long)GTAG << 32) | (unsigned long long)__float_as_uint(t);
            __hip_atomic_store((gu64*)(gran + ((rt * 8 + ct) * 2 + w) * 64 + tok), g, __ATOMIC_RELAXED, __HIP_MEMORY_SCOPE_AGENT);
        }
        {
            unsigned long long g0, g1;
            gu64* p0 = (gu64*)(gran + ((rt * 8 + w) * 2 + 0) * 64 + tok);
            gu64* p1 = (gu64*)(gran + ((rt * 8 + w) * 2 + 1) * 64 + tok);
            for (unsigned spins = 0; spins < (1u << 22); ++spins) {
                g0 = __hip_atomic_load(p0, __ATOMIC_RELAXED, __HIP_MEMORY_SCOPE_AGENT);
                g1 = __hip_atomic_load(p1, __ATOMIC_RELAXED, __HIP_MEMORY_SCOPE_AGENT);
                bool ok = ((u32)(g0 >> 32) == GTAG) && ((u32)(g1 >> 32) == GTAG);
                if (__all(ok)) break;
                __builtin_amdgcn_s_sleep(1);
            }
            red2[w * 64 + tok] = __uint_as_float((u32)g0);
            red2[512 + w * 64 + tok] = __uint_as_float((u32)g1);
        }
        __syncthreads();
        float a = 0.f, b = 0.f;
#pragma unroll
        for (int c = 0; c < 8; ++c) { a += red2[c * 64 + tok]; b += red2[512 + c * 64 + tok]; }
        const float mean = a * (1.0f / 640.0f);
        float var = b * (1.0f / 640.0f) - mean * mean;
        var = var > 0.f ? var : 0.f;
        float mb, rinv;
        ln_finish(mean, var, mb, rinv);
#pragma unroll
        for (int it = 0; it < 2; ++it) {
            int cc = w + 8 * it;
            if (cc < 10) {
                u32x4 o;
#pragma unroll
                for (int e = 0; e < 4; ++e) {
                    float xa = lo2f(x2[it][e]), xb = hi2f(x2[it][e]);
                    float qa = rbf(rbf(xa - mb) * rinv), qb = rbf(rbf(xb - mb) * rinv);
                    float ga = rbf(qa * lo2f(yv[it][e])), gb = rbf(qb * hi2f(yv[it][e]));
                    o[e] = pack2(xa + ga, xb + gb);
                }
                *(u32x4*)(xout + xf_idx(token, 80 * ct + 8 * cc)) = o;
            }
        }
    }
}

extern "C" void kernel_launch(void* const* d_in, const int* in_sizes, int n_in,
                              void* d_out, int out_size, void* d_ws, size_t ws_size,
                              hipStream_t stream) {
    const u16* q = (const u16*)d_in[0];
    const u16* k = (const u16*)d_in[1];
    const u16* v = (const u16*)d_in[2];
    const u16* sk = (const u16*)d_in[3];
    const u16* ssmk = (const u16*)d_in[4];
    const u16* ssmD = (const u16*)d_in[5];
    const u16* wg = (const u16*)d_in[7];
    const u16* wu = (const u16*)d_in[8];
    const u16* wd = (const u16*)d_in[9];
    u16* out = (u16*)d_out;

    char* ws = (char*)d_ws;
    constexpr size_t SZ_X = (size_t)S_ * D_ * 2;
    size_t off = 0;
    u16* W1p = (u16*)(ws + off); off += (size_t)NUNIT * KS1 * 1024;
    u16* kvR = (u16*)(ws + off); off += SZ_X;
    u16* yF = (u16*)(ws + off); off += SZ_X;
    u16* xaF = (u16*)(ws + off); off += SZ_X;
    u16* xbF = (u16*)(ws + off); off += SZ_X;
    u16* qF = (u16*)(ws + off); off += SZ_X;
    u16* W2p = (u16*)(ws + off); off += (size_t)40 * KS2 * 1024;
    u16* hp = (u16*)(ws + off); off += (size_t)128 * KS2 * 1024;
    f32x2* qs = (f32x2*)(ws + off); off += (size_t)S_ * 8;
    float* cs = (float*)(ws + off); off += (size_t)NUNIT * 32 * 4;
    unsigned long long* gran = (unsigned long long*)(ws + off); off += (size_t)GRAN_WORDS * 8;

    prep_kernel<<<320 + 512, 256, 0, stream>>>(k, v, sk, kvR, q, qF, qs, (u32x4*)gran);
    conv_kernel<<<NCONV + NREP1 + NREP2 + NCS, 256, 0, stream>>>(kvR, ssmk, ssmD, yF, wg, wu, wd, W1p, W2p, qF, qs, xaF, cs);
    gemm1_kernel<<<256, 512, 0, stream>>>(xaF, W1p, cs, hp);
    gemm2_kernel<true><<<256, 512, 0, stream>>>(hp, W2p, xaF, yF, xbF, gran);
    gemm1_kernel<<<256, 512, 0, stream>>>(xbF, W1p, cs, hp);
    gemm2_kernel<false><<<256, 512, 0, stream>>>(hp, W2p, xbF, yF, out, gran);
}
```

```cpp
#include <hip/hip_runtime.h>
#include <hip/hip_bf16.h>
#include <stdint.h>

typedef unsigned short u16;
typedef unsigned int u32;
typedef __attribute__((ext_vector_type(8))) short bf16x8;
typedef __attribute__((ext_vector_type(4))) float f32x4;
typedef __attribute__((ext_vector_type(16))) float f32x16;
typedef __attribute__((ext_vector_type(4))) u32 u32x4;
typedef __attribute__((ext_vector_type(2))) u32 u32x2;
typedef __attribute__((ext_vector_type(2))) float f32x2;
typedef __attribute__((ext_vector_type(2))) __bf16 bf16x2v;

constexpr int S_ = 2048, D_ = 640, F_ = 1728;
constexpr int NUNIT = 108;
constexpr int KS1 = 40;
constexpr int KS2 = 54;

__device__ __forceinline__ float lo2f(u32 v) { return __uint_as_float(v << 16); }
__device__ __forceinline__ float hi2f(u32 v) { return __uint_as_float(v & 0xffff0000u); }
__device__ __forceinline__ u32 pack2(float a, float b) {
    f32x2 f = {a, b};
    bf16x2v r = __builtin_convertvector(f, bf16x2v);
    return __builtin_bit_cast(u32, r);
}
__device__ __forceinline__ float rbf(float a) {
    __bf16 b = (__bf16)a;
    return __uint_as_float(((u32)__builtin_bit_cast(u16, b)) << 16);
}
__device__ __forceinline__ u16 f2bf(float a) {
    __bf16 b = (__bf16)a;
    return __builtin_bit_cast(u16, b);
}

__device__ __forceinline__ float rowsum16(float x) {
    x += __builtin_bit_cast(float, __builtin_amdgcn_update_dpp(0, __builtin_bit_cast(int, x), 0x128, 0xf, 0xf, false));
    x += __builtin_bit_cast(float, __builtin_amdgcn_update_dpp(0, __builtin_bit_cast(int, x), 0x124, 0xf, 0xf, false));
    x += __builtin_bit_cast(float, __builtin_amdgcn_update_dpp(0, __builtin_bit_cast(int, x), 0x122, 0xf, 0xf, false));
    x += __builtin_bit_cast(float, __builtin_amdgcn_update_dpp(0, __builtin_bit_cast(int, x), 0x121, 0xf, 0xf, false));
    return x;
}

__device__ __forceinline__ float wavesum(float x) {
    x = rowsum16(x);
    x += __shfl_xor(x, 16);
    x += __shfl_xor(x, 32);
    return x;
}
__device__ __forceinline__ long xf_idx(int token, int d) {
    return ((long)((token >> 5) * 80 + (d >> 3)) * 32 + (token & 31)) * 8 + (d & 7);
}
__device__ __forceinline__ float dot2bf(u32 a, u32 b, float c) {
    return __builtin_amdgcn_fdot2_f32_bf16(__builtin_bit_cast(bf16x2v, a), __builtin_bit_cast(bf16x2v, b), c, false);
}
__device__ __forceinline__ void ln_finish(float mean, float var, float& mb, float& rinv) {
    mb = rbf(mean);
    float den = rbf(__builtin_sqrtf(rbf(rbf(var) + 1e-5f)));
    rinv = 1.0f / den;
}
constexpr u32 GTAG = 0x13572468u;
constexpr int GRAN_WORDS = 32 * 8 * 2 * 64;

constexpr int NREP1 = 40;
constexpr int NREP2 = 27;
constexpr int NCS = 27;
__global__ __launch_bounds__(256) void prep_kernel(const u16* __restrict__ k, const u16* __restrict__ v,
                                                   const u16* __restrict__ sk, u16* __restrict__ kvR,
                                                   const u16* __restrict__ q, u16* __restrict__ qF,
                                                   f32x2* __restrict__ qs, u32x4* __restrict__ gran) {
    __shared__ __attribute__((aligned(16))) u32 T[64 * 32];
    if (blockIdx.x >= 320) {
        const int b = blockIdx.x - 320;
        if (b < 64) { u32x4 z = {0u, 0u, 0u, 0u}; gran[b * 256 + threadIdx.x] = z; }
        const int lane = threadIdx.x & 63;
        const int row = b * 4 + (threadIdx.x >> 6);
        const u32* xs = (const u32*)(q + (long)row * D_);
        u32 xv[5];
#pragma unroll
        for (int m = 0; m < 5; ++m) xv[m] = xs[lane + 64 * m];
        float sum = 0.f;
#pragma unroll
        for (int m = 0; m < 5; ++m) sum += lo2f(xv[m]) + hi2f(xv[m]);
        sum = wavesum(sum);
        float mean = sum * (1.0f / 640.0f);
        float sq = 0.f;
#pragma unroll
        for (int m = 0; m < 5; ++m) {
            float a = lo2f(xv[m]) - mean, c = hi2f(xv[m]) - mean;
            sq += a * a + c * c;
        }
        sq = wavesum(sq);
        float mb, rinv;
        ln_finish(mean, sq * (1.0f / 640.0f), mb, rinv);
        if (lane == 0) { f32x2 o = {mb, rinv}; qs[row] = o; }
#pragma unroll
        for (int m = 0; m < 5; ++m) *(u32*)(qF + xf_idx(row, 2 * (lane + 64 * m))) = xv[m];
        return;
    }
    const int st = blockIdx.x / 10, dt = blockIdx.x % 10;
    const int s0 = st * 64, d0 = dt * 64;
    const int t = threadIdx.x, dc = t & 7, rp = t >> 3;
    const int s = s0 + 2 * rp, d = d0 + 8 * dc;
    u32x4 kr[5];
#pragma unroll
    for (int i = 0; i < 5; ++i) {
        int row = s - 3 + i;
        u32x4 z = {0u, 0u, 0u, 0u};
        kr[i] = (row >= 0) ? *(const u32x4*)(k + (long)row * D_ + d) : z;
    }
    u32x4 v0 = *(const u32x4*)(v + (long)s * D_ + d);
    u32x4 v1 = *(const u32x4*)(v + (long)(s + 1) * D_ + d);
    u32x4 skv[4];
#pragma unroll
    for (int i = 0; i < 4; ++i) skv[i] = *(const u32x4*)(sk + (long)d * 4 + 8 * i);
    const int p = 31 - rp;
#pragma unroll
    for (int e = 0; e < 8; ++e) {
        u32 w01 = skv[e >> 1][(e & 1) * 2 + 0], w23 = skv[e >> 1][(e & 1) * 2 + 1];
        float c0 = lo2f(w01), c1 = hi2f(w01), c2 = lo2f(w23), c3 = hi2f(w23);
        float kk[5];
#pragma unroll
        for (int i = 0; i < 5; ++i) {
            u32 wv = kr[i][e >> 1];
            kk[i] = (e & 1) ? hi2f(wv) : lo2f(wv);
        }
        float va = (e & 1) ? hi2f(v0[e >> 1]) : lo2f(v0[e >> 1]);
        float vb = (e & 1) ? hi2f(v1[e >> 1]) : lo2f(v1[e >> 1]);
        float a = rbf(kk[0] * c3);
        a = rbf(a + rbf(kk[1] * c2));
        a = rbf(a + rbf(kk[2] * c1));
        a = rbf(a + rbf(kk[3] * c0));
        float b = rbf(kk[1] * c3);
        b = rbf(b + rbf(kk[2] * c2));
        b = rbf(b + rbf(kk[3] * c1));
        b = rbf(b + rbf(kk[4] * c0));
        u32 pk = pack2(b * vb, a * va);
        int dl = 8 * dc + e;
        T[dl * 32 + ((((p >> 2) ^ dc)) << 2) + (p & 3)] = pk;
    }
    __syncthreads();
#pragma unroll
    for (int it = 0; it < 2; ++it) {
        int idx = t + 256 * it;
        int dl = idx >> 3, c = idx & 7;
        u32x4 o = *(const u32x4*)&T[dl * 32 + ((c ^ (dl >> 3)) << 2)];
        *(u32x4*)(kvR + (long)(d0 + dl) * S_ + (1984 - s0) + 8 * c) = o;
    }
}

constexpr int HC_STRIDE = 4416;
constexpr int KV_BYTES = 47 * 144;
constexpr int WAVE_LDS = 4 * HC_STRIDE + KV_BYTES;
constexpr int YBUF_OFF = 4 * WAVE_LDS;
constexpr int YCH = 32 * 136;
constexpr int CONV_LDS = YBUF_OFF + 4 * YCH;
constexpr int NCONV = 160;

__global__ __launch_bounds__(256, 1) void conv_kernel(const u16* __restrict__ kvR, const u16* __restrict__ ssmk,
                                                      const u16* __restrict__ ssmD, u16* __restrict__ yF,
                                                      const u16* __restrict__ qF,
                                                      const f32x2* __restrict__ qs, u16* __restrict__ xaF,
                                                      const u16* __restrict__ wg, const u16* __restrict__ wu,
                                                      const u16* __restrict__ wd, u16* __restrict__ W1p,
                                                      u16* __restrict__ W2p, float* __restrict__ cs) {
    __shared__ __attribute__((aligned(16))) char lds[CONV_LDS];
    const int tid = threadIdx.x, lane = tid & 63, w = tid >> 6;
    const int bid = blockIdx.x;
    if (bid < NCONV) {
        const int ch = bid * 4 + w;
        char* wl = lds + w * WAVE_LDS;
        char* kvl = wl + 4 * HC_STRIDE;
        const u32x4* hsrc = (const u32x4*)(ssmk + (long)ch * S_);
        const u32x4* ksrc = (const u32x4*)(kvR + (long)ch * S_);
        u32x4 kvv[4], ha[5], hb[5];
#pragma unroll
        for (int m = 0; m < 5; ++m) {
            int c = lane - 8 + 64 * m;
            int c0 = c < 0 ? 0 : (c > 255 ? 255 : c);
            int c1 = c + 1 < 0 ? 0 : (c + 1 > 255 ? 255 : c + 1);
            ha[m] = hsrc[c0];
            hb[m] = hsrc[c1];
        }
#pragma unroll
        for (int m = 0; m < 4; ++m) kvv[m] = ksrc[lane + 64 * m];
#pragma unroll
        for (int m = 0; m < 5; ++m) {
            int c = lane - 8 + 64 * m;
            u32x4 z = {0u, 0u, 0u, 0u};
            u32x4 a = (c >= 0) ? ha[m] : z;
            u32x4 b = (c + 1 >= 0 && c + 1 < 256) ? hb[m] : z;
            u32 ww[8] = {a[0], a[1], a[2], a[3], b[0], b[1], b[2], b[3]};
            u32x4 o0, o1, o2, o3;
#pragma unroll
            for (int i = 0; i < 4; ++i) {
                o0[i] = __builtin_amdgcn_alignbit(ww[i + 1], ww[i], 16);
                o1[i] = ww[i + 1];
                o2[i] = __builtin_amdgcn_alignbit(ww[i + 2], ww[i + 1], 16);
                o3[i] = ww[i + 2];
            }
            if (m < 4 || c < 256) {
                char* dst = wl + (c + 8) * 16;
                *(u32x4*)(dst + 0 * HC_STRIDE) = o0;
                *(u32x4*)(dst + 1 * HC_STRIDE) = o1;
                *(u32x4*)(dst + 2 * HC_STRIDE) = o2;
                *(u32x4*)(dst + 3 * HC_STRIDE) = o3;
            }
        }
#pragma unroll
        for (int m = 0; m < 4; ++m) {
            int c = lane + 64 * m;
            *(u32x4*)(kvl + (c >> 3) * 144 + (c & 7) * 16) = kvv[m];
        }
        {
            u32x4 z = {0u, 0u, 0u, 0u};
#pragma unroll
            for (int m = 0; m < 3; ++m) {
                int c = lane + 64 * m;
                if (c < 135) *(u32x4*)(kvl + 32 * 144 + c * 16) = z;
            }
        }
        __builtin_amdgcn_s_waitcnt(0xC07F);
        __builtin_amdgcn_wave_barrier();
        u32x2 qv[8];
        f32x2 qst[8];
#pragma unroll
        for (int it = 0; it < 8; ++it) {
            int s = tid + 256 * it;
            qv[it] = *(const u32x2*)(qF + xf_idx(s, bid * 4));
            qst[it] = qs[s];
        }

        const int r = lane & 15, kq = lane >> 4;
        u32 aoffA = (u32)(w * WAVE_LDS + (r & 3) * HC_STRIDE + 2 * ((r & 12) + 8 * kq));
        u32 aoffB = aoffA + 8, aoffC = aoffA + 32, aoffD = aoffA + 40;
        asm volatile("" : "+v"(aoffB));
        asm volatile("" : "+v"(aoffC));
        asm volatile("" : "+v"(aoffD));
        const u32 boff = (u32)(w * WAVE_LDS + 4 * HC_STRIDE + 16 * (9 * (31 - r) + kq));

        f32x4 acc[2][4];
#pragma unroll
        for (int n = 0; n < 2; ++n)
#pragma unroll
            for (int m = 0; m < 4; ++m) acc[n][m] = (f32x4){0.f, 0.f, 0.f, 0.f};

        bf16x8 A[136], B0[32], B1[64];
#define LOADA(i) do { \
            u32x2 l0 = *(const u32x2*)(lds + (((i) & 1) ? aoffC : aoffA) + 64 * ((i) >> 1)); \
            u32x2 l1 = *(const u32x2*)(lds + (((i) & 1) ? aoffD : aoffB) + 64 * ((i) >> 1)); \
            u32x4 t4 = {l0[0], l0[1], l1[0], l1[1]}; \
            A[i] = __builtin_bit_cast(bf16x8, t4); } while (0)
#define KSO(ks) (16 * (9 * ((ks) >> 1) + 4 * ((ks) & 1)))
#define LOADB(ks) do { \
            B1[ks] = *(const bf16x8*)(lds + boff + KSO(ks) - 16 * 9 * 16); \
            if ((ks) < 32) B0[(ks) < 32 ? (ks) : 0] = *(const bf16x8*)(lds + boff + KSO(ks)); } while (0)
#pragma unroll
        for (int i = 0; i < 6; ++i) LOADA(i);
        LOADB(0);
        LOADB(1);
        __builtin_amdgcn_sched_barrier(0);
#pragma unroll
        for (int ks = 0; ks < 64; ++ks) {
            if (ks + 2 < 64) {
                LOADA(2 * ks + 6);
                LOADA(2 * ks + 7);
                LOADB(ks + 2);
            }
            __builtin_amdgcn_sched_barrier(0);
#pragma unroll
            for (int m = 0; m < 4; ++m)
                acc[1][m] = __builtin_amdgcn_mfma_f32_16x16x32_bf16(A[2 * ks + m], B1[ks], acc[1][m], 0, 0, 0);
            if (ks < 32) {
#pragma unroll
                for (int m = 0; m < 4; ++m)
                    acc[0][m] = __builtin_amdgcn_mfma_f32_16x16x32_bf16(A[2 * ks + m], B0[ks < 32 ? ks : 0], acc[0][m], 0, 0, 0);
            }
            __builtin_amdgcn_sched_barrier(0);
        }
#undef LOADA
#undef LOADB
#undef KSO
        const float Dv = lo2f((u32)ssmD[ch]);
        char* ybuf = lds + YBUF_OFF;
#pragma unroll
        for (int n = 0; n < 2; ++n)
#pragma unroll
            for (int m = 0; m < 4; ++m) {
                int a = 16 * n + r;
                int ib = 60 - 16 * m - 4 * kq;
                u32x2 kk = *(const u32x2*)(kvl + (31 - a) * 144 + ib * 2);
                float kvf[4] = {hi2f(kk[1]), lo2f(kk[1]), hi2f(kk[0]), lo2f(kk[0])};
                float yo[4];
#pragma unroll
                for (int j = 0; j < 4; ++j) yo[j] = rbf(acc[n][m][j]) + rbf(kvf[j] * Dv);
                u32x2 o = {pack2(yo[0], yo[1]), pack2(yo[2], yo[3])};
                *(u32x2*)(ybuf + w * YCH + a * 136 + (16 * m + 4 * kq) * 2) = o;
            }
        __syncthreads();
#pragma unroll
        for (int it = 0; it < 8; ++it) {
            int s = tid + 256 * it;
            const char* yp = ybuf + (s >> 6) * 136 + (s & 63) * 2;
            u32x2 yo;
            yo[0] = (u32)*(const u16*)(yp) | ((u32)*(const u16*)(yp + YCH) << 16);
            yo[1] = (u32)*(const u16*)(yp + 2 * YCH) | ((u32)*(const u16*)(yp + 3 * YCH) << 16);
            float mb = qst[it][0], rinv = qst[it][1];
            u32x2 xo;
#pragma unroll
            for (int j = 0; j < 2; ++j) {
                float a = lo2f(qv[it][j]), b = hi2f(qv[it][j]);
                float qa = rbf(rbf(a - mb) * rinv), qb = rbf(rbf(b - mb) * rinv);
                float ga = rbf(qa * lo2f(yo[j])), gb = rbf(qb * hi2f(yo[j]));
                xo[j] = pack2(a + ga, b + gb);
            }
            long idx = xf_idx(s, bid * 4);
            *(u32x2*)(yF + idx) = yo;
            *(u32x2*)(xaF + idx) = xo;
        }
    } else if (bid < NCONV + NREP1) {
        const int ks = bid - NCONV;
        u16* T = (u16*)lds;
        constexpr int TS = 1736;
        u32x4 t[27];
#pragma unroll
        for (int it = 0; it < 27; ++it) {
            int i = tid + 256 * it;
            int mat = i / 3456, rem = i % 3456, kr = rem / 216, cc = rem % 216;
            t[it] = *(const u32x4*)((mat ? wu : wg) + (long)(16 * ks + kr) * F_ + 8 * cc);
        }
#pragma unroll
        for (int it = 0; it < 27; ++it) {
            int i = tid + 256 * it;
            int mat = i / 3456, rem = i % 3456, kr = rem / 216, cc = rem % 216;
            *(u32x4*)(T + (mat * 16 + kr) * TS + 8 * cc) = t[it];
        }
        __syncthreads();
#pragma unroll 9
        for (int it = 0; it < 27; ++it) {
            int i = tid + 256 * it;
            int uu = i >> 6, l = i & 63;
            int ii = l & 31, hh = l >> 5;
            int mat = ii >> 4, col = 16 * uu + (ii & 15);
            const u16* tp = T + (mat * 16 + 8 * hh) * TS + col;
            u32x4 o;
#pragma unroll
            for (int j = 0; j < 4; ++j) o[j] = (u32)tp[(2 * j) * TS] | ((u32)tp[(2 * j + 1) * TS] << 16);
            *(u32x4*)(W1p + ((long)(uu * KS1 + ks) * 64 + l) * 8) = o;
        }
    } else if (bid < NCONV + NREP1 + NREP2) {
        const int s0 = 2 * (bid - NCONV - NREP1);
        u16* T = (u16*)lds;
        constexpr int TS = 648;
        u32x4 t[20];
#pragma unroll
        for (int it = 0; it < 20; ++it) {
            int i = tid + 256 * it;
            int fr = i / 80, cc = i % 80;
            t[it] = *(const u32x4*)(wd + (long)(32 * s0 + fr) * D_ + 8 * cc);
        }
#pragma unroll
        for (int it = 0; it < 20; ++it) {
            int i = tid + 256 * it;
            int fr = i / 80, cc = i % 80;
            *(u32x4*)(T + fr * TS + 8 * cc) = t[it];
        }
        __syncthreads();
#pragma unroll 10
        for (int it = 0; it < 20; ++it) {
            int i = tid + 256 * it;
            int sl = i / 2560, rem = i % 2560;
            int nt = rem >> 6, l = rem & 63;
            int idx = l & 15, kq = l >> 4;
            int fb = 32 * sl + 16 * (kq >> 1) + 4 * (kq & 1);
            const u16* tp = T + 16 * nt + idx;
            u32x4 o;
            o[0] = (u32)tp[(fb + 0) * TS] | ((u32)tp[(fb + 1) * TS] << 16);
            o[1] = (u32)tp[(fb + 2) * TS] | ((u32)tp[(fb + 3) * TS] << 16);
            o[2] = (u32)tp[(fb + 8) * TS] | ((u32)tp[(fb + 9) * TS] << 16);
            o[3] = (u32)tp[(fb + 10) * TS] | ((u32)tp[(fb + 11) * TS] << 16);
            *(u32x4*)(W2p + ((long)(nt * KS2 + s0 + sl) * 64 + l) * 8) = o;
        }
    } else {
        const int cb = bid - NCONV - NREP1 - NREP2;
        const int cp = tid & 31, kg = tid >> 5;
        float a0 = 0.f, a1 = 0.f, b0 = 0.f, b1 = 0.f;
#pragma unroll 20
        for (int i = 0; i < 80; ++i) {
            u32 wv = *(const u32*)(wg + (long)(kg * 80 + i) * F_ + cb * 64 + 2 * cp);
            u32 uv = *(const u32*)(wu + (long)(kg * 80 + i) * F_ + cb * 64 + 2 * cp);
            a0 += lo2f(wv); a1 += hi2f(wv);
            b0 += lo2f(uv); b1 += hi2f(uv);
        }
        float* T = (float*)lds;
        T[kg * 64 + 2 * cp] = a0;
        T[kg * 64 + 2 * cp + 1] = a1;
        T[512 + kg * 64 + 2 * cp] = b0;
        T[512 + kg * 64 + 2 * cp + 1] = b1;
        __syncthreads();
        if (tid < 128) {
            int mat = tid >> 6, c = tid & 63;
            float t = 0.f;
#pragma unroll
            for (int g = 0; g < 8; ++g) t += T[mat * 512 + g * 64 + c];
            int f = cb * 64 + c;
            cs[(f >> 4) * 32 + mat * 16 + (f & 15)] = t;
        }
    }
}

constexpr int G1_STAGE = 44 * 1024;
constexpr int G1_CS_OFF = 3 * G1_STAGE;

__device__ __forceinline__ void g1_epi_unit(const f32x16& a, const char* cp, float mb, float rinv, u16* hp,
                                            int token, int u, int hh) {
    f32x4 c0 = *(const f32x4*)(cp), c1 = *(const f32x4*)(cp + 32);
    f32x4 c2 = *(const f32x4*)(cp + 64), c3 = *(const f32x4*)(cp + 96);
    float hv[8];
#pragma unroll
    for (int e = 0; e < 8; ++e) {
        float cg = (e < 4) ? c0[e & 3] : c1[e & 3];
        float cu = (e < 4) ? c2[e & 3] : c3[e & 3];
        u32 gu = pack2((a[e] - mb * cg) * rinv, (a[e + 8] - mb * cu) * rinv);
        float g = lo2f(gu), uu = hi2f(gu);
        float sg = rbf(__builtin_amdgcn_rcpf(1.0f + __expf(-g)));
        hv[e] = sg * uu;
    }
    u32x4 o = {pack2(hv[0], hv[1]), pack2(hv[2], hv[3]), pack2(hv[4], hv[5]), pack2(hv[6], hv[7])};
    long idx = ((long)((token >> 4) * KS2 + (u >> 1)) * 64 + (token & 15) + 16 * (2 * (u & 1) + hh));
    *(u32x4*)(hp + idx * 8) = o;
}

constexpr int G1_ST_OFF = G1_CS_OFF + 1024;
__device__ __forceinline__ void g1_stats(const char* lds, int tb, int tk, float& mb, float& rinv) {
    f32x2 a = *(const f32x2*)(lds + G1_ST_OFF + (tb * 64 + tk) * 8);
    f32x2 b = *(const f32x2*)(lds + G1_ST_OFF + (tb * 64 + tk + 32) * 8);
    f32x2 c = *(const f32x2*)(lds + G1_ST_OFF + 2048 + (tb * 64 + tk) * 8);
    f32x2 d = *(const f32x2*)(lds + G1_ST_OFF + 2048 + (tb * 64 + tk + 32) * 8);
    float s1 = (a[0] + b[0]) + (c[0] + d[0]), s2 = (a[1] + b[1]) + (c[1] + d[1]);
    const float mean = s1 * (1.0f / 640.0f);
    float var = s2 * (1.0f / 640.0f) - mean * mean;
    var = var > 0.f ? var : 0.f;
    ln_finish(mean, var, mb, rinv);
}

__global__ __launch_bounds__(512, 2) void gemm1_kernel(const u16* __restrict__ xF, const u16* __restrict__ W1p,
                                                       const float* __restrict__ cs, u16* __restrict__ hp) {
    __shared__ __attribute__((aligned(16))) char lds[3 * G1_STAGE + 1024 + 4096];
    const int tid = threadIdx.x, lane = tid & 63;
    const int w = __builtin_amdgcn_readfirstlane(tid >> 6);
    const int xcd = blockIdx.x & 7, jj = blockIdx.x >> 3;
    const int rt = 8 * (xcd >> 2) + (jj & 7), ct = 4 * (xcd & 3) + (jj >> 3);
    const int ubase = ct * 7;
    const int tb = w & 3;
    const int tk = lane & 31, hh = lane >> 5;
    const int token = 128 * rt + 32 * tb + tk;
    u32 lb[3];
#pragma unroll
    for (int b = 0; b < 3; ++b) { lb[b] = (u32)(b * G1_STAGE + lane * 16); asm volatile("" : "+v"(lb[b])); }

    if (w >= 4) {
        const int lw = w - 4;
        const char* base = (const char*)W1p;
        const u32 xoff = (u32)((const char*)xF - base);
        u32 src[11];
#pragma unroll
        for (int i = 0; i < 11; ++i) {
            int p = lw * 11 + i;
            int stream = p >> 2, ksl = p & 3;
            int uu = ubase + stream - 4;
            if (uu > NUNIT - 1) uu = NUNIT - 1;
            src[i] = (stream < 4) ? xoff + (u32)((((4 * rt + stream) * KS1 + ksl) * 64 + lane) * 16)
                                  : (u32)(((uu * KS1 + ksl) * 64 + lane) * 16);
        }
#define G1_ISSUE(s) do { \
        _Pragma("unroll") for (int i = 0; i < 11; ++i) \
            __builtin_amdgcn_global_load_lds((const u32*)(base + (src[i] + (u32)((s) * 4096))), \
                (u32*)(lds + ((s) % 3) * G1_STAGE + (lw * 11 + i) * 1024 + lane * 16), 16, 0, 0); \
        } while (0)
        G1_ISSUE(0);
        G1_ISSUE(1);
        float sa[4] = {0.f, 0.f, 0.f, 0.f}, sb[4] = {0.f, 0.f, 0.f, 0.f};
#pragma unroll
        for (int s = 0; s < 10; ++s) {
            if (s < 9) asm volatile("s_waitcnt vmcnt(11) lgkmcnt(0)" ::: "memory");
            else asm volatile("s_waitcnt vmcnt(0) lgkmcnt(0)" ::: "memory");
            __builtin_amdgcn_s_barrier();
            asm volatile("" ::: "memory");
            u32x4 xx[2];
#pragma unroll
            for (int j = 0; j < 2; ++j) xx[j] = *(const u32x4*)(lds + lb[s % 3] + tb * 4096 + j * 1024);
            if (s + 2 < 10) G1_ISSUE(s + 2);
#pragma unroll
            for (int j = 0; j < 2; ++j)
#pragma unroll
                for (int e = 0; e < 4; ++e) {
                    float lo = lo2f(xx[j][e]), hi = hi2f(xx[j][e]);
                    sa[e] += lo + hi;
                    sb[e] = __builtin_fmaf(lo, lo, __builtin_fmaf(hi, hi, sb[e]));
                }
        }
#undef G1_ISSUE
        float s1 = (sa[0] + sa[1]) + (sa[2] + sa[3]), s2 = (sb[0] + sb[1]) + (sb[2] + sb[3]);
        { f32x2 st = {s1, s2}; *(f32x2*)(lds + G1_ST_OFF + (tb * 64 + lane) * 8) = st; }
        __syncthreads();
        float mb, rinv;
        g1_stats(lds, tb, tk, mb, rinv);
#pragma unroll
        for (int i = 4; i < 7; ++i) {
            int u = ubase + i;
            if (u < NUNIT) {
                f32x16 a;
#pragma unroll
                for (int r = 0; r < 4; ++r) {
                    f32x4 t = *(const f32x4*)(lds + G1_STAGE + (tb * 12 + (i - 4) * 4 + r) * 1024 + lane * 16);
                    a[4 * r] = t[0]; a[4 * r + 1] = t[1]; a[4 * r + 2] = t[2]; a[4 * r + 3] = t[3];
                }
                g1_epi_unit(a, lds + G1_CS_OFF + i * 128 + hh * 16, mb, rinv, hp, token, u, hh);
            }
        }
        return;
    }
    if (lane < 56) {
        int e = ubase * 32 + w * 56 + lane;
        float csv = (e < NUNIT * 32) ? cs[e] : 0.f;
        *(float*)(lds + G1_CS_OFF + (w * 56 + lane) * 4) = csv;
    }
    f32x16 acc[7];
#pragma unroll
    for (int a = 0; a < 7; ++a)
#pragma unroll
        for (int i = 0; i < 16; ++i) acc[a][i] = 0.f;
    bf16x8 xf[3], wf[3][7];
    float sa[2] = {0.f, 0.f}, sb[2] = {0.f, 0.f};
#pragma unroll
    for (int g = 0; g < 42; ++g) {
        const int s = g >> 2, j = g & 3;
        if (g < 40) {
            if (j == 0) {
                asm volatile("s_waitcnt lgkmcnt(0)" ::: "memory");
                __builtin_amdgcn_s_barrier();
                asm volatile("" ::: "memory");
            }
            xf[g % 3] = *(const bf16x8*)(lds + lb[s % 3] + tb * 4096 + j * 1024);
#pragma unroll
            for (int i = 0; i < 7; ++i) wf[g % 3][i] = *(const bf16x8*)(lds + lb[s % 3] + (4 + i) * 4096 + j * 1024);
        }
        if (g >= 2) {
            const int c = (g - 2) % 3;
#pragma unroll
            for (int i = 0; i < 7; ++i)
                acc[i] = __builtin_amdgcn_mfma_f32_32x32x16_bf16(wf[c][i], xf[c], acc[i], 0, 0, 0);
            if (((g - 2) & 3) >= 2) {
                u32x4 xx = __builtin_bit_cast(u32x4, xf[c]);
#pragma unroll
                for (int e = 0; e < 4; ++e) {
                    float lo = lo2f(xx[e]), hi = hi2f(xx[e]);
                    sa[e & 1] += lo + hi;
                    sb[e & 1] = __builtin_fmaf(lo, lo, __builtin_fmaf(hi, hi, sb[e & 1]));
                }
            }
#pragma unroll
            for (int i = 0; i < 7; ++i) {
                __builtin_amdgcn_sched_group_barrier(0x8, 1, 0);
                __builtin_amdgcn_sched_group_barrier(0x100, 1, 0);
                __builtin_amdgcn_sched_group_barrier(0x2, 4, 0);
            }
        }
        __builtin_amdgcn_sched_barrier(0);
    }
#pragma unroll
    for (int i = 4; i < 7; ++i)
#pragma unroll
        for (int r = 0; r < 4; ++r) {
            f32x4 t = {acc[i][4 * r], acc[i][4 * r + 1], acc[i][4 * r + 2], acc[i][4 * r + 3]};
            *(f32x4*)(lds + G1_STAGE + (tb * 12 + (i - 4) * 4 + r) * 1024 + lane * 16) = t;
        }
    { f32x2 st = {sa[0] + sa[1], sb[0] + sb[1]}; *(f32x2*)(lds + G1_ST_OFF + 2048 + (tb * 64 + lane) * 8) = st; }
    __syncthreads();
    float mb, rinv;
    g1_stats(lds, tb, tk, mb, rinv);
#pragma unroll
    for (int i = 0; i < 4; ++i) {
        int u = ubase + i;
        if (u < NUNIT) g1_epi_unit(acc[i], lds + G1_CS_OFF + i * 128 + hh * 16, mb, rinv, hp, token, u, hh);
    }
}

constexpr int PROW = 336;
constexpr int PTILE = 64 * PROW;
constexpr int PD2 = 3;
typedef __attribute__((address_space(1))) unsigned long long gu64;

template <bool MID>
__global__ __launch_bounds__(512, 2) void gemm2_kernel(const u16* __restrict__ hp, const u16* __restrict__ W2p,
                                                       const u16* __restrict__ xresF, const u16* __restrict__ yF,
                                                       u16* __restrict__ xout, unsigned long long* gran) {
    __shared__ __attribute__((aligned(16))) char lds[4 * PTILE];
    const int tid = threadIdx.x, lane = tid & 63, w = tid >> 6;
    const int xcd = blockIdx.x & 7, jj = blockIdx.x >> 3;
    const int rt = 8 * (xcd >> 1) + (jj & 7), ct = 4 * (xcd & 1) + (jj >> 3);

    const bf16x8* hb = (const bf16x8*)hp + (long)(4 * rt) * KS2 * 64 + lane;
    const bf16x8* wbp = (const bf16x8*)W2p + (long)(5 * ct) * KS2 * 64 + lane;
    const int nsteps = (w < 6) ? 7 : 6;

    bf16x8 hf[PD2][4], wf[PD2][5];
#pragma unroll
    for (int i = 0; i < PD2; ++i) {
        int s = w + 8 * i;
#pragma unroll
        for (int m = 0; m < 4; ++m) hf[i][m] = hb[(long)(m * KS2 + s) * 64];
#pragma unroll
        for (int n = 0; n < 5; ++n) wf[i][n] = wbp[(long)(n * KS2 + s) * 64];
    }
    const int tok = lane;
    const int token = 64 * rt + tok;
    u32x4 res[2], yv[2];
#pragma unroll
    for (int it = 0; it < 2; ++it) {
        int cc = w + 8 * it;
        if (cc < 10) {
            long idx = xf_idx(token, 80 * ct + 8 * cc);
            res[it] = *(const u32x4*)(xresF + idx);
            if (MID) yv[it] = *(const u32x4*)(yF + idx);
        }
    }
    f32x4 acc[5][4];
#pragma unroll
    for (int n = 0; n < 5; ++n)
#pragma unroll
        for (int m = 0; m < 4; ++m) acc[n][m] = (f32x4){0.f, 0.f, 0.f, 0.f};
#pragma unroll
    for (int i = 0; i < 7; ++i) {
        if (i < nsteps) {
#pragma unroll
            for (int n = 0; n < 5; ++n)
#pragma unroll
                for (int m = 0; m < 4; ++m)
                    acc[n][m] = __builtin_amdgcn_mfma_f32_16x16x32_bf16(wf[i % PD2][n], hf[i % PD2][m], acc[n][m], 0, 0, 0);
            if (i + PD2 < nsteps) {
                int s = w + 8 * (i + PD2);
#pragma unroll
                for (int m = 0; m < 4; ++m) hf[i % PD2][m] = hb[(long)(m * KS2 + s) * 64];
#pragma unroll
                for (int n = 0; n < 5; ++n) wf[i % PD2][n] = wbp[(long)(n * KS2 + s) * 64];
            }
        }
    }
    const int tl = lane & 15, q = lane >> 4;
    char* slot = lds + (w & 3) * PTILE + tl * PROW + q * 16;
    if (w >= 4) {
#pragma unroll
        for (int n = 0; n < 5; ++n)
#pragma unroll
            for (int m = 0; m < 4; ++m) *(f32x4*)(slot + m * 16 * PROW + n * 64) = acc[n][m];
    }
    __syncthreads();
    if (w < 4) {
#pragma unroll
        for (int n = 0; n < 5; ++n)
#pragma unroll
            for (int m = 0; m < 4; ++m) {
                f32x4 o = *(const f32x4*)(slot + m * 16 * PROW + n * 64);
                *(f32x4*)(slot + m * 16 * PROW + n * 64) = o + acc[n][m];
            }
    }
    __syncthreads();
    u32x4 x2[2];
#pragma unroll
    for (int it = 0; it < 2; ++it) {
        int cc = w + 8 * it;
        if (cc < 10) {
            const char* p = lds + tok * PROW + cc * 32;
            f32x4 s0 = *(const f32x4*)(p), s1 = *(const f32x4*)(p + 16);
#pragma unroll
            for (int k = 1; k < 4; ++k) {
                s0 += *(const f32x4*)(p + k * PTILE);
                s1 += *(const f32x4*)(p + k * PTILE + 16);
            }
            u32x4 rs = res[it];
            x2[it][0] = pack2(lo2f(rs[0]) + rbf(s0[0]), hi2f(rs[0]) + rbf(s0[1]));
            x2[it][1] = pack2(lo2f(rs[1]) + rbf(s0[2]), hi2f(rs[1]) + rbf(s0[3]));
            x2[it][2] = pack2(lo2f(rs[2]) + rbf(s1[0]), hi2f(rs[2]) + rbf(s1[1]));
            x2[it][3] = pack2(lo2f(rs[3]) + rbf(s1[2]), hi2f(rs[3]) + rbf(s1[3]));
            if (!MID) *(u32x4*)(xout + (long)token * D_ + 80 * ct + 8 * cc) = x2[it];
        }
    }
    if (MID) {
        __syncthreads();
        float* red = (float*)lds;
        float* red2 = (float*)(lds + 8192);
#pragma unroll
        for (int it = 0; it < 2; ++it) {
            int cc = w + 8 * it;
            if (cc < 10) {
                float a = 0.f, b = 0.f;
#pragma unroll
                for (int e = 0; e < 4; ++e) { a = dot2bf(x2[it][e], 0x3f803f80u, a); b = dot2bf(x2[it][e], x2[it][e], b); }
                red[cc * 64 + tok] = a;
                red[640 + cc * 64 + tok] = b;
            }
        }
        __syncthreads();
        if (w < 2) {
            float t = 0.f;
#pragma unroll
            for (int c = 0; c < 10; ++c) t += red[w * 640 + c * 64 + tok];
            unsigned long long g = ((unsigned long long)GTAG << 32) | (unsigned long long)__float_as_uint(t);
            __hip_atomic_store((gu64*)(gran + ((rt * 8 + ct) * 2 + w) * 64 + tok), g, __ATOMIC_RELAXED, __HIP_MEMORY_SCOPE_AGENT);
        }
        {
            unsigned long long g0, g1;
            gu64* p0 = (gu64*)(gran + ((rt * 8 + w) * 2 + 0) * 64 + tok);
            gu64* p1 = (gu64*)(gran + ((rt * 8 + w) * 2 + 1) * 64 + tok);
            for (unsigned spins = 0; spins < (1u << 22); ++spins) {
                g0 = __hip_atomic_load(p0, __ATOMIC_RELAXED, __HIP_MEMORY_SCOPE_AGENT);
                g1 = __hip_atomic_load(p1, __ATOMIC_RELAXED, __HIP_MEMORY_SCOPE_AGENT);
                bool ok = ((u32)(g0 >> 32) == GTAG) && ((u32)(g1 >> 32) == GTAG);
                if (__all(ok)) break;
                __builtin_amdgcn_s_sleep(1);
            }
            red2[w * 64 + tok] = __uint_as_float((u32)g0);
            red2[512 + w * 64 + tok] = __uint_as_float((u32)g1);
        }
        __syncthreads();
        float a = 0.f, b = 0.f;
#pragma unroll
        for (int c = 0; c < 8; ++c) { a += red2[c * 64 + tok]; b += red2[512 + c * 64 + tok]; }
        const float mean = a * (1.0f / 640.0f);
        float var = b * (1.0f / 640.0f) - mean * mean;
        var = var > 0.f ? var : 0.f;
        float mb, rinv;
        ln_finish(mean, var, mb, rinv);
#pragma unroll
        for (int it = 0; it < 2; ++it) {
            int cc = w + 8 * it;
            if (cc < 10) {
                u32x4 o;
#pragma unroll
                for (int e = 0; e < 4; ++e) {
                    float xa = lo2f(x2[it][e]), xb = hi2f(x2[it][e]);
                    float qa = rbf(rbf(xa - mb) * rinv), qb = rbf(rbf(xb - mb) * rinv);
                    float ga = rbf(qa * lo2f(yv[it][e])), gb = rbf(qb * hi2f(yv[it][e]));
                    o[e] = pack2(xa + ga, xb + gb);
                }
                *(u32x4*)(xout + xf_idx(token, 80 * ct + 8 * cc)) = o;
            }
        }
    }
}

extern "C" void kernel_launch(void* const* d_in, const int* in_sizes, int n_in,
                              void* d_out, int out_size, void* d_ws, size_t ws_size,
                              hipStream_t stream) {
    const u16* q = (const u16*)d_in[0];
    const u16* k = (const u16*)d_in[1];
    const u16* v = (const u16*)d_in[2];
    const u16* sk = (const u16*)d_in[3];
    const u16* ssmk = (const u16*)d_in[4];
    const u16* ssmD = (const u16*)d_in[5];
    const u16* wg = (const u16*)d_in[7];
    const u16* wu = (const u16*)d_in[8];
    const u16* wd = (const u16*)d_in[9];
    u16* out = (u16*)d_out;

    char* ws = (char*)d_ws;
    constexpr size_t SZ_X = (size_t)S_ * D_ * 2;
    size_t off = 0;
    u16* W1p = (u16*)(ws + off); off += (size_t)NUNIT * KS1 * 1024;
    u16* kvR = (u16*)(ws + off); off += SZ_X;
    u16* yF = (u16*)(ws + off); off += SZ_X;
    u16* xaF = (u16*)(ws + off); off += SZ_X;
    u16* xbF = (u16*)(ws + off); off += SZ_X;
    u16* qF = (u16*)(ws + off); off += SZ_X;
    u16* W2p = (u16*)(ws + off); off += (size_t)40 * KS2 * 1024;
    u16* hp = (u16*)(ws + off); off += (size_t)128 * KS2 * 1024;
    f32x2* qs = (f32x2*)(ws + off); off += (size_t)S_ * 8;
    float* cs = (float*)(ws + off); off += (size_t)NUNIT * 32 * 4;
    unsigned long long* gran = (unsigned long long*)(ws + off); off += (size_t)GRAN_WORDS * 8;

    prep_kernel<<<320 + 512, 256, 0, stream>>>(k, v, sk, kvR, q, qF, qs, (u32x4*)gran);
    conv_kernel<<<NCONV + NREP1 + NREP2 + NCS, 256, 0, stream>>>(kvR, ssmk, ssmD, yF, qF, qs, xaF, wg, wu, wd, W1p, W2p, cs);
    gemm1_kernel<<<256, 512, 0, stream>>>(xaF, W1p, cs, hp);
    gemm2_kernel<true><<<256, 512, 0, stream>>>(hp, W2p, xaF, yF, xbF, gran);
    gemm1_kernel<<<256, 512, 0, stream>>>(xbF, W1p, cs, hp);
    gemm2_kernel<false><<<256, 512, 0, stream>>>(hp, W2p, xbF, yF, out, gran);
}
```

```cpp
#include <hip/hip_runtime.h>
#include <hip/hip_bf16.h>
#include <stdint.h>

typedef unsigned short u16;
typedef unsigned int u32;
typedef __attribute__((ext_vector_type(8))) short bf16x8;
typedef __attribute__((ext_vector_type(4))) float f32x4;
typedef __attribute__((ext_vector_type(16))) float f32x16;
typedef __attribute__((ext_vector_type(4))) u32 u32x4;
typedef __attribute__((ext_vector_type(2))) u32 u32x2;
typedef __attribute__((ext_vector_type(2))) float f32x2;
typedef __attribute__((ext_vector_type(2))) __bf16 bf16x2v;

constexpr int S_ = 2048, D_ = 640, F_ = 1728;
constexpr int NUNIT = 108;
constexpr int KS1 = 40;
constexpr int KS2 = 54;

__device__ __forceinline__ float lo2f(u32 v) { return __uint_as_float(v << 16); }
__device__ __forceinline__ float hi2f(u32 v) { return __uint_as_float(v & 0xffff0000u); }
__device__ __forceinline__ u32 pack2(float a, float b) {
    f32x2 f = {a, b};
    bf16x2v r = __builtin_convertvector(f, bf16x2v);
    return __builtin_bit_cast(u32, r);
}
__device__ __forceinline__ float rbf(float a) {
    __bf16 b = (__bf16)a;
    return __uint_as_float(((u32)__builtin_bit_cast(u16, b)) << 16);
}
__device__ __forceinline__ u16 f2bf(float a) {
    __bf16 b = (__bf16)a;
    return __builtin_bit_cast(u16, b);
}

__device__ __forceinline__ float rowsum16(float x) {
    x += __builtin_bit_cast(float, __builtin_amdgcn_update_dpp(0, __builtin_bit_cast(int, x), 0x128, 0xf, 0xf, false));
    x += __builtin_bit_cast(float, __builtin_amdgcn_update_dpp(0, __builtin_bit_cast(int, x), 0x124, 0xf, 0xf, false));
    x += __builtin_bit_cast(float, __builtin_amdgcn_update_dpp(0, __builtin_bit_cast(int, x), 0x122, 0xf, 0xf, false));
    x += __builtin_bit_cast(float, __builtin_amdgcn_update_dpp(0, __builtin_bit_cast(int, x), 0x121, 0xf, 0xf, false));
    return x;
}

__device__ __forceinline__ float wavesum(float x) {
    x = rowsum16(x);
    x += __shfl_xor(x, 16);
    x += __shfl_xor(x, 32);
    return x;
}
__device__ __forceinline__ long xf_idx(int token, int d) {
    return ((long)((token >> 5) * 80 + (d >> 3)) * 32 + (token & 31)) * 8 + (d & 7);
}
__device__ __forceinline__ float dot2bf(u32 a, u32 b, float c) {
    return __builtin_amdgcn_fdot2_f32_bf16(__builtin_bit_cast(bf16x2v, a), __builtin_bit_cast(bf16x2v, b), c, false);
}
__device__ __forceinline__ void ln_finish(float mean, float var, float& mb, float& rinv) {
    mb = rbf(mean);
    float den = rbf(__builtin_sqrtf(rbf(rbf(var) + 1e-5f)));
    rinv = 1.0f / den;
}
constexpr u32 GTAG = 0x13572468u;
constexpr int GRAN_WORDS = 32 * 8 * 2 * 64;

constexpr int NREP1 = 40;
constexpr int NREP2 = 27;
constexpr int NCS = 27;
__global__ __launch_bounds__(256) void prep_kernel(const u16* __restrict__ k, const u16* __restrict__ v,
                                                   const u16* __restrict__ sk, u16* __restrict__ kvR,
                                                   const u16* __restrict__ q, u16* __restrict__ qF,
                                                   f32x2* __restrict__ qs, u32x4* __restrict__ gran) {
    __shared__ __attribute__((aligned(16))) u32 T[64 * 32];
    if (blockIdx.x >= 320) {
        const int b = blockIdx.x - 320;
        if (b < 66) { u32x4 z = {0u, 0u, 0u, 0u}; gran[b * 256 + threadIdx.x] = z; }
        const int lane = threadIdx.x & 63;
        const int row = b * 4 + (threadIdx.x >> 6);
        const u32* xs = (const u32*)(q + (long)row * D_);
        u32 xv[5];
#pragma unroll
        for (int m = 0; m < 5; ++m) xv[m] = xs[lane + 64 * m];
        float sum = 0.f;
#pragma unroll
        for (int m = 0; m < 5; ++m) sum += lo2f(xv[m]) + hi2f(xv[m]);
        sum = wavesum(sum);
        float mean = sum * (1.0f / 640.0f);
        float sq = 0.f;
#pragma unroll
        for (int m = 0; m < 5; ++m) {
            float a = lo2f(xv[m]) - mean, c = hi2f(xv[m]) - mean;
            sq += a * a + c * c;
        }
        sq = wavesum(sq);
        float mb, rinv;
        ln_finish(mean, sq * (1.0f / 640.0f), mb, rinv);
        if (lane == 0) { f32x2 o = {mb, rinv}; qs[row] = o; }
#pragma unroll
        for (int m = 0; m < 5; ++m) *(u32*)(qF + xf_idx(row, 2 * (lane + 64 * m))) = xv[m];
        return;
    }
    const int st = blockIdx.x / 10, dt = blockIdx.x % 10;
    const int s0 = st * 64, d0 = dt * 64;
    const int t = threadIdx.x, dc = t & 7, rp = t >> 3;
    const int s = s0 + 2 * rp, d = d0 + 8 * dc;
    u32x4 kr[5];
#pragma unroll
    for (int i = 0; i < 5; ++i) {
        int row = s - 3 + i;
        u32x4 z = {0u, 0u, 0u, 0u};
        kr[i] = (row >= 0) ? *(const u32x4*)(k + (long)row * D_ + d) : z;
    }
    u32x4 v0 = *(const u32x4*)(v + (long)s * D_ + d);
    u32x4 v1 = *(const u32x4*)(v + (long)(s + 1) * D_ + d);
    u32x4 skv[4];
#pragma unroll
    for (int i = 0; i < 4; ++i) skv[i] = *(const u32x4*)(sk + (long)d * 4 + 8 * i);
    const int p = 31 - rp;
#pragma unroll
    for (int e = 0; e < 8; ++e) {
        u32 w01 = skv[e >> 1][(e & 1) * 2 + 0], w23 = skv[e >> 1][(e & 1) * 2 + 1];
        float c0 = lo2f(w01), c1 = hi2f(w01), c2 = lo2f(w23), c3 = hi2f(w23);
        float kk[5];
#pragma unroll
        for (int i = 0; i < 5; ++i) {
            u32 wv = kr[i][e >> 1];
            kk[i] = (e & 1) ? hi2f(wv) : lo2f(wv);
        }
        float va = (e & 1) ? hi2f(v0[e >> 1]) : lo2f(v0[e >> 1]);
        float vb = (e & 1) ? hi2f(v1[e >> 1]) : lo2f(v1[e >> 1]);
        float a = rbf(kk[0] * c3);
        a = rbf(a + rbf(kk[1] * c2));
        a = rbf(a + rbf(kk[2] * c1));
        a = rbf(a + rbf(kk[3] * c0));
        float b = rbf(kk[1] * c3);
        b = rbf(b + rbf(kk[2] * c2));
        b = rbf(b + rbf(kk[3] * c1));
        b = rbf(b + rbf(kk[4] * c0));
        u32 pk = pack2(b * vb, a * va);
        int dl = 8 * dc + e;
        T[dl * 32 + ((((p >> 2) ^ dc)) << 2) + (p & 3)] = pk;
    }
    __syncthreads();
#pragma unroll
    for (int it = 0; it < 2; ++it) {
        int idx = t + 256 * it;
        int dl = idx >> 3, c = idx & 7;
        u32x4 o = *(const u32x4*)&T[dl * 32 + ((c ^ (dl >> 3)) << 2)];
        *(u32x4*)(kvR + (long)(d0 + dl) * S_ + (1984 - s0) + 8 * c) = o;
    }
}

constexpr int HC_STRIDE = 4416;
constexpr int KV_BYTES = 47 * 144;
constexpr int WAVE_LDS = 4 * HC_STRIDE + KV_BYTES;
constexpr int YBUF_OFF = 4 * WAVE_LDS;
constexpr int YCH = 32 * 136;
constexpr int CONV_LDS = YBUF_OFF + 4 * YCH;
constexpr int NCONV = 160;

__global__ __launch_bounds__(256, 1) void conv_kernel(const u16* __restrict__ kvR, const u16* __restrict__ ssmk,
                                                      const u16* __restrict__ ssmD, u16* __restrict__ yF,
                                                      const u16* __restrict__ qF,
                                                      const f32x2* __restrict__ qs, u16* __restrict__ xaF,
                                                      const u16* __restrict__ wg, const u16* __restrict__ wu,
                                                      const u16* __restrict__ wd, u16* __restrict__ W1p,
                                                      u16* __restrict__ W2p, float* __restrict__ cs) {
    __shared__ __attribute__((aligned(16))) char lds[CONV_LDS];
    const int tid = threadIdx.x, lane = tid & 63, w = tid >> 6;
    const int bid = blockIdx.x;
    if (bid < NCONV) {
        const int ch = bid * 4 + w;
        char* wl = lds + w * WAVE_LDS;
        char* kvl = wl + 4 * HC_STRIDE;
        const u32x4* hsrc = (const u32x4*)(ssmk + (long)ch * S_);
        const u32x4* ksrc = (const u32x4*)(kvR + (long)ch * S_);
        u32x4 kvv[4], ha[5], hb[5];
#pragma unroll
        for (int m = 0; m < 5; ++m) {
            int c = lane - 8 + 64 * m;
            int c0 = c < 0 ? 0 : (c > 255 ? 255 : c);
            int c1 = c + 1 < 0 ? 0 : (c + 1 > 255 ? 255 : c + 1);
            ha[m] = hsrc[c0];
            hb[m] = hsrc[c1];
        }
#pragma unroll
        for (int m = 0; m < 4; ++m) kvv[m] = ksrc[lane + 64 * m];
#pragma unroll
        for (int m = 0; m < 5; ++m) {
            int c = lane - 8 + 64 * m;
            u32x4 z = {0u, 0u, 0u, 0u};
            u32x4 a = (c >= 0) ? ha[m] : z;
            u32x4 b = (c + 1 >= 0 && c + 1 < 256) ? hb[m] : z;
            u32 ww[8] = {a[0], a[1], a[2], a[3], b[0], b[1], b[2], b[3]};
            u32x4 o0, o1, o2, o3;
#pragma unroll
            for (int i = 0; i < 4; ++i) {
                o0[i] = __builtin_amdgcn_alignbit(ww[i + 1], ww[i], 16);
                o1[i] = ww[i + 1];
                o2[i] = __builtin_amdgcn_alignbit(ww[i + 2], ww[i + 1], 16);
                o3[i] = ww[i + 2];
            }
            if (m < 4 || c < 256) {
                char* dst = wl + (c + 8) * 16;
                *(u32x4*)(dst + 0 * HC_STRIDE) = o0;
                *(u32x4*)(dst + 1 * HC_STRIDE) = o1;
                *(u32x4*)(dst + 2 * HC_STRIDE) = o2;
                *(u32x4*)(dst + 3 * HC_STRIDE) = o3;
            }
        }
#pragma unroll
        for (int m = 0; m < 4; ++m) {
            int c = lane + 64 * m;
            *(u32x4*)(kvl + (c >> 3) * 144 + (c & 7) * 16) = kvv[m];
        }
        {
            u32x4 z = {0u, 0u, 0u, 0u};
#pragma unroll
            for (int m = 0; m < 3; ++m) {
                int c = lane + 64 * m;
                if (c < 135) *(u32x4*)(kvl + 32 * 144 + c * 16) = z;
            }
        }
        __builtin_amdgcn_s_waitcnt(0xC07F);
        __builtin_amdgcn_wave_barrier();
        u32x2 qv[8];
        f32x2 qst[8];
#pragma unroll
        for (int it = 0; it < 8; ++it) {
            int s = tid + 256 * it;
            qv[it] = *(const u32x2*)(qF + xf_idx(s, bid * 4));
            qst[it] = qs[s];
        }

        const int r = lane & 15, kq = lane >> 4;
        u32 aoffA = (u32)(w * WAVE_LDS + (r & 3) * HC_STRIDE + 2 * ((r & 12) + 8 * kq));
        u32 aoffB = aoffA + 8, aoffC = aoffA + 32, aoffD = aoffA + 40;
        asm volatile("" : "+v"(aoffB));
        asm volatile("" : "+v"(aoffC));
        asm volatile("" : "+v"(aoffD));
        const u32 boff = (u32)(w * WAVE_LDS + 4 * HC_STRIDE + 16 * (9 * (31 - r) + kq));

        f32x4 acc[2][4];
#pragma unroll
        for (int n = 0; n < 2; ++n)
#pragma unroll
            for (int m = 0; m < 4; ++m) acc[n][m] = (f32x4){0.f, 0.f, 0.f, 0.f};

        bf16x8 A[136], B0[32], B1[64];
#define LOADA(i) do { \
            u32x2 l0 = *(const u32x2*)(lds + (((i) & 1) ? aoffC : aoffA) + 64 * ((i) >> 1)); \
            u32x2 l1 = *(const u32x2*)(lds + (((i) & 1) ? aoffD : aoffB) + 64 * ((i) >> 1)); \
            u32x4 t4 = {l0[0], l0[1], l1[0], l1[1]}; \
            A[i] = __builtin_bit_cast(bf16x8, t4); } while (0)
#define KSO(ks) (16 * (9 * ((ks) >> 1) + 4 * ((ks) & 1)))
#define LOADB(ks) do { \
            B1[ks] = *(const bf16x8*)(lds + boff + KSO(ks) - 16 * 9 * 16); \
            if ((ks) < 32) B0[(ks) < 32 ? (ks) : 0] = *(const bf16x8*)(lds + boff + KSO(ks)); } while (0)
#pragma unroll
        for (int i = 0; i < 6; ++i) LOADA(i);
        LOADB(0);
        LOADB(1);
        __builtin_amdgcn_sched_barrier(0);
#pragma unroll
        for (int ks = 0; ks < 64; ++ks) {
            if (ks + 2 < 64) {
                LOADA(2 * ks + 6);
                LOADA(2 * ks + 7);
                LOADB(ks + 2);
            }
            __builtin_amdgcn_sched_barrier(0);
#pragma unroll
            for (int m = 0; m < 4; ++m)
                acc[1][m] = __builtin_amdgcn_mfma_f32_16x16x32_bf16(A[2 * ks + m], B1[ks], acc[1][m], 0, 0, 0);
            if (ks < 32) {
#pragma unroll
                for (int m = 0; m < 4; ++m)
                    acc[0][m] = __builtin_amdgcn_mfma_f32_16x16x32_bf16(A[2 * ks + m], B0[ks < 32 ? ks : 0], acc[0][m], 0, 0, 0);
            }
            __builtin_amdgcn_sched_barrier(0);
        }
#undef LOADA
#undef LOADB
#undef KSO
        const float Dv = lo2f((u32)ssmD[ch]);
        char* ybuf = lds + YBUF_OFF;
#pragma unroll
        for (int n = 0; n < 2; ++n)
#pragma unroll
            for (int m = 0; m < 4; ++m) {
                int a = 16 * n + r;
                int ib = 60 - 16 * m - 4 * kq;
                u32x2 kk = *(const u32x2*)(kvl + (31 - a) * 144 + ib * 2);
                float kvf[4] = {hi2f(kk[1]), lo2f(kk[1]), hi2f(kk[0]), lo2f(kk[0])};
                float yo[4];
#pragma unroll
                for (int j = 0; j < 4; ++j) yo[j] = rbf(acc[n][m][j]) + rbf(kvf[j] * Dv);
                u32x2 o = {pack2(yo[0], yo[1]), pack2(yo[2], yo[3])};
                *(u32x2*)(ybuf + w * YCH + a * 136 + (16 * m + 4 * kq) * 2) = o;
            }
        __syncthreads();
#pragma unroll
        for (int it = 0; it < 8; ++it) {
            int s = tid + 256 * it;
            const char* yp = ybuf + (s >> 6) * 136 + (s & 63) * 2;
            u32x2 yo;
            yo[0] = (u32)*(const u16*)(yp) | ((u32)*(const u16*)(yp + YCH) << 16);
            yo[1] = (u32)*(const u16*)(yp + 2 * YCH) | ((u32)*(const u16*)(yp + 3 * YCH) << 16);
            float mb = qst[it][0], rinv = qst[it][1];
            u32x2 xo;
#pragma unroll
            for (int j = 0; j < 2; ++j) {
                float a = lo2f(qv[it][j]), b = hi2f(qv[it][j]);
                float qa = rbf(rbf(a - mb) * rinv), qb = rbf(rbf(b - mb) * rinv);
                float ga = rbf(qa * lo2f(yo[j])), gb = rbf(qb * hi2f(yo[j]));
                xo[j] = pack2(a + ga, b + gb);
            }
            long idx = xf_idx(s, bid * 4);
            *(u32x2*)(yF + idx) = yo;
            *(u32x2*)(xaF + idx) = xo;
        }
    } else if (bid < NCONV + NREP1) {
        const int ks = bid - NCONV;
        u16* T = (u16*)lds;
        constexpr int TS = 1736;
        u32x4 t[27];
#pragma unroll
        for (int it = 0; it < 27; ++it) {
            int i = tid + 256 * it;
            int mat = i / 3456, rem = i % 3456, kr = rem / 216, cc = rem % 216;
            t[it] = *(const u32x4*)((mat ? wu : wg) + (long)(16 * ks + kr) * F_ + 8 * cc);
        }
#pragma unroll
        for (int it = 0; it < 27; ++it) {
            int i = tid + 256 * it;
            int mat = i / 3456, rem = i % 3456, kr = rem / 216, cc = rem % 216;
            *(u32x4*)(T + (mat * 16 + kr) * TS + 8 * cc) = t[it];
        }
        __syncthreads();
#pragma unroll 9
        for (int it = 0; it < 27; ++it) {
            int i = tid + 256 * it;
            int uu = i >> 6, l = i & 63;
            int ii = l & 31, hh = l >> 5;
            int mat = ii >> 4, col = 16 * uu + (ii & 15);
            const u16* tp = T + (mat * 16 + 8 * hh) * TS + col;
            u32x4 o;
#pragma unroll
            for (int j = 0; j < 4; ++j) o[j] = (u32)tp[(2 * j) * TS] | ((u32)tp[(2 * j + 1) * TS] << 16);
            *(u32x4*)(W1p + ((long)(uu * KS1 + ks) * 64 + l) * 8) = o;
        }
    } else if (bid < NCONV + NREP1 + NREP2) {
        const int s0 = 2 * (bid - NCONV - NREP1);
        u16* T = (u16*)lds;
        constexpr int TS = 648;
        u32x4 t[20];
#pragma unroll
        for (int it = 0; it < 20; ++it) {
            int i = tid + 256 * it;
            int fr = i / 80, cc = i % 80;
            t[it] = *(const u32x4*)(wd + (long)(32 * s0 + fr) * D_ + 8 * cc);
        }
#pragma unroll
        for (int it = 0; it < 20; ++it) {
            int i = tid + 256 * it;
            int fr = i / 80, cc = i % 80;
            *(u32x4*)(T + fr * TS + 8 * cc) = t[it];
        }
        __syncthreads();
#pragma unroll 10
        for (int it = 0; it < 20; ++it) {
            int i = tid + 256 * it;
            int sl = i / 2560, rem = i % 2560;
            int nt = rem >> 6, l = rem & 63;
            int idx = l & 15, kq = l >> 4;
            int fb = 32 * sl + 16 * (kq >> 1) + 4 * (kq & 1);
            const u16* tp = T + 16 * nt + idx;
            u32x4 o;
            o[0] = (u32)tp[(fb + 0) * TS] | ((u32)tp[(fb + 1) * TS] << 16);
            o[1] = (u32)tp[(fb + 2) * TS] | ((u32)tp[(fb + 3) * TS] << 16);
            o[2] = (u32)tp[(fb + 8) * TS] | ((u32)tp[(fb + 9) * TS] << 16);
            o[3] = (u32)tp[(fb + 10) * TS] | ((u32)tp[(fb + 11) * TS] << 16);
            *(u32x4*)(W2p + ((long)(nt * KS2 + s0 + sl) * 64 + l) * 8) = o;
        }
    } else {
        const int cb = bid - NCONV - NREP1 - NREP2;
        const int cp = tid & 31, kg = tid >> 5;
        float a0 = 0.f, a1 = 0.f, b0 = 0.f, b1 = 0.f;
#pragma unroll 20
        for (int i = 0; i < 80; ++i) {
            u32 wv = *(const u32*)(wg + (long)(kg * 80 + i) * F_ + cb * 64 + 2 * cp);
            u32 uv = *(const u32*)(wu + (long)(kg * 80 + i) * F_ + cb * 64 + 2 * cp);
            a0 += lo2f(wv); a1 += hi2f(wv);
            b0 += lo2f(uv); b1 += hi2f(uv);
        }
        float* T = (float*)lds;
        T[kg * 64 + 2 * cp] = a0;
        T[kg * 64 + 2 * cp + 1] = a1;
        T[512 + kg * 64 + 2 * cp] = b0;
        T[512 + kg * 64 + 2 * cp + 1] = b1;
        __syncthreads();
        if (tid < 128) {
            int mat = tid >> 6, c = tid & 63;
            float t = 0.f;
#pragma unroll
            for (int g = 0; g < 8; ++g) t += T[mat * 512 + g * 64 + c];
            int f = cb * 64 + c;
            cs[(f >> 4) * 32 + mat * 16 + (f & 15)] = t;
        }
    }
}

constexpr int G1_STAGE = 44 * 1024;
constexpr int G1_CS_OFF = 3 * G1_STAGE;

typedef __attribute__((address_space(1))) unsigned long long gu64;
typedef __attribute__((address_space(1))) unsigned int gu32;
typedef __amdgpu_buffer_rsrc_t rsrc_t;
__device__ __forceinline__ rsrc_t mk_rsrc(const void* p, u32 bytes) {
    return __builtin_amdgcn_make_buffer_rsrc((void*)p, 0, bytes, 0x00020000);
}
__device__ __forceinline__ void st16_dev(rsrc_t r, u32 off, u32x4 v) { __builtin_amdgcn_raw_buffer_store_b128(v, r, off, 0, 16); }
__device__ __forceinline__ u32x4 ld16_dev(rsrc_t r, u32 off) { return __builtin_amdgcn_raw_buffer_load_b128(r, off, 0, 16); }

__device__ __forceinline__ void wait_count(u32* cnt, u32 target) {
    for (unsigned spins = 0; spins < (1u << 22); ++spins) {
        u32 v = __hip_atomic_load((gu32*)cnt, __ATOMIC_RELAXED, __HIP_MEMORY_SCOPE_AGENT);
        if (v >= target) break;
        __builtin_amdgcn_s_sleep(2);
    }
    __builtin_amdgcn_fence(__ATOMIC_ACQUIRE, "wavefront");
    asm volatile("" ::: "memory");
}
__device__ __forceinline__ void signal_count(u32* cnt) {
    asm volatile("s_waitcnt vmcnt(0)" ::: "memory");
    __syncthreads();
    if (threadIdx.x == 0) __hip_atomic_fetch_add((gu32*)cnt, 1u, __ATOMIC_RELAXED, __HIP_MEMORY_SCOPE_AGENT);
}

__device__ __forceinline__ void g1_epi_unit(const f32x16& a, const char* cp, float mb, float rinv, rsrc_t hr,
                                            int token, int u, int hh) {
    f32x4 c0 = *(const f32x4*)(cp), c1 = *(const f32x4*)(cp + 32);
    f32x4 c2 = *(const f32x4*)(cp + 64), c3 = *(const f32x4*)(cp + 96);
    float hv[8];
#pragma unroll
    for (int e = 0; e < 8; ++e) {
        float cg = (e < 4) ? c0[e & 3] : c1[e & 3];
        float cu = (e < 4) ? c2[e & 3] : c3[e & 3];
        u32 gu = pack2((a[e] - mb * cg) * rinv, (a[e + 8] - mb * cu) * rinv);
        float g = lo2f(gu), uu = hi2f(gu);
        float sg = rbf(__builtin_amdgcn_rcpf(1.0f + __expf(-g)));
        hv[e] = sg * uu;
    }
    u32x4 o = {pack2(hv[0], hv[1]), pack2(hv[2], hv[3]), pack2(hv[4], hv[5]), pack2(hv[6], hv[7])};
    u32 idx = (u32)(((token >> 4) * KS2 + (u >> 1)) * 64 + (token & 15) + 16 * (2 * (u & 1) + hh));
    st16_dev(hr, idx * 16, o);
}

constexpr int G1_ST_OFF = G1_CS_OFF + 1024;
constexpr int FFN_LDS = 3 * G1_STAGE + 1024 + 4096;
constexpr u32 HP_BYTES = 128u * KS2 * 1024u;
constexpr u32 X_BYTES = (u32)S_ * D_ * 2u;
__device__ __forceinline__ void g1_stats(const char* lds, int tb, int tk, float& mb, float& rinv) {
    f32x2 a = *(const f32x2*)(lds + G1_ST_OFF + (tb * 64 + tk) * 8);
    f32x2 b = *(const f32x2*)(lds + G1_ST_OFF + (tb * 64 + tk + 32) * 8);
    f32x2 c = *(const f32x2*)(lds + G1_ST_OFF + 2048 + (tb * 64 + tk) * 8);
    f32x2 d = *(const f32x2*)(lds + G1_ST_OFF + 2048 + (tb * 64 + tk + 32) * 8);
    float s1 = (a[0] + b[0]) + (c[0] + d[0]), s2 = (a[1] + b[1]) + (c[1] + d[1]);
    const float mean = s1 * (1.0f / 640.0f);
    float var = s2 * (1.0f / 640.0f) - mean * mean;
    var = var > 0.f ? var : 0.f;
    ln_finish(mean, var, mb, rinv);
}

__device__ __forceinline__ void g1_phase(const bool WAIT, char* lds, const u16* __restrict__ xF, const u16* __restrict__ W1p,
                                         const float* __restrict__ cs, u16* __restrict__ hp, u32* wcnt) {
    const int tid = threadIdx.x, lane = tid & 63;
    const int w = __builtin_amdgcn_readfirstlane(tid >> 6);
    const int xcd = blockIdx.x & 7, jj = blockIdx.x >> 3;
    const int rt = 8 * (xcd >> 2) + (jj & 7), ct = 4 * (xcd & 3) + (jj >> 3);
    const int ubase = ct * 7;
    const int tb = w & 3;
    const int tk = lane & 31, hh = lane >> 5;
    const int token = 128 * rt + 32 * tb + tk;
    const rsrc_t hr = mk_rsrc(hp, HP_BYTES);
    u32 lb[3];
#pragma unroll
    for (int b = 0; b < 3; ++b) { lb[b] = (u32)(b * G1_STAGE + lane * 16); asm volatile("" : "+v"(lb[b])); }

    if (w >= 4) {
        const int lw = w - 4;
        const char* wbase = (const char*)W1p;
        const char* xbase = (const char*)xF;
        u32 srcw[7];
        const u32 srcx = (u32)((((4 * rt + lw) * KS1) * 64 + lane) * 16);
#pragma unroll
        for (int i = 0; i < 7; ++i) {
            int q = lw * 7 + i;
            int uu = ubase + (q >> 2);
            if (uu > NUNIT - 1) uu = NUNIT - 1;
            srcw[i] = (u32)(((uu * KS1 + (q & 3)) * 64 + lane) * 16);
        }
#define G1_ISSUE_W(s) do { \
        _Pragma("unroll") for (int i = 0; i < 7; ++i) \
            __builtin_amdgcn_global_load_lds((const u32*)(wbase + (srcw[i] + (u32)((s) * 4096))), \
                (u32*)(lds + ((s) % 3) * G1_STAGE + (16 + lw * 7 + i) * 1024 + lane * 16), 16, 0, 0); \
        } while (0)
#define G1_ISSUE_X(s) do { \
        _Pragma("unroll") for (int i = 0; i < 4; ++i) \
            __builtin_amdgcn_global_load_lds((const u32*)(xbase + (srcx + (u32)((s) * 4096 + i * 1024))), \
                (u32*)(lds + ((s) % 3) * G1_STAGE + (lw * 4 + i) * 1024 + lane * 16), 16, 0, 16); \
        } while (0)
        G1_ISSUE_W(0);
        G1_ISSUE_W(1);
        if (WAIT) wait_count(wcnt, 16);
        G1_ISSUE_X(0);
        G1_ISSUE_X(1);
        float sa[4] = {0.f, 0.f, 0.f, 0.f}, sb[4] = {0.f, 0.f, 0.f, 0.f};
#pragma unroll
        for (int s = 0; s < 10; ++s) {
            if (s == 0) asm volatile("s_waitcnt vmcnt(4) lgkmcnt(0)" ::: "memory");
            else if (s < 9) asm volatile("s_waitcnt vmcnt(11) lgkmcnt(0)" ::: "memory");
            else asm volatile("s_waitcnt vmcnt(0) lgkmcnt(0)" ::: "memory");
            __builtin_amdgcn_s_barrier();
            asm volatile("" ::: "memory");
            u32x4 xx[2];
#pragma unroll
            for (int j = 0; j < 2; ++j) xx[j] = *(const u32x4*)(lds + lb[s % 3] + tb * 4096 + j * 1024);
            if (s + 2 < 10) { G1_ISSUE_W(s + 2); G1_ISSUE_X(s + 2); }
#pragma unroll
            for (int j = 0; j < 2; ++j)
#pragma unroll
                for (int e = 0; e < 4; ++e) {
                    float lo = lo2f(xx[j][e]), hi = hi2f(xx[j][e]);
                    sa[e] += lo + hi;
                    sb[e] = __builtin_fmaf(lo, lo, __builtin_fmaf(hi, hi, sb[e]));
                }
        }
#undef G1_ISSUE_W
#undef G1_ISSUE_X
        float s1 = (sa[0] + sa[1]) + (sa[2] + sa[3]), s2 = (sb[0] + sb[1]) + (sb[2] + sb[3]);
        { f32x2 st = {s1, s2}; *(f32x2*)(lds + G1_ST_OFF + (tb * 64 + lane) * 8) = st; }
        __syncthreads();
        float mb, rinv;
        g1_stats(lds, tb, tk, mb, rinv);
#pragma unroll
        for (int i = 4; i < 7; ++i) {
            int u = ubase + i;
            if (u < NUNIT) {
                f32x16 a;
#pragma unroll
                for (int r = 0; r < 4; ++r) {
                    f32x4 t = *(const f32x4*)(lds + G1_STAGE + (tb * 12 + (i - 4) * 4 + r) * 1024 + lane * 16);
                    a[4 * r] = t[0]; a[4 * r + 1] = t[1]; a[4 * r + 2] = t[2]; a[4 * r + 3] = t[3];
                }
                g1_epi_unit(a, lds + G1_CS_OFF + i * 128 + hh * 16, mb, rinv, hr, token, u, hh);
            }
        }
    } else {
        if (lane < 56) {
            int e = ubase * 32 + w * 56 + lane;
            float csv = (e < NUNIT * 32) ? cs[e] : 0.f;
            *(float*)(lds + G1_CS_OFF + (w * 56 + lane) * 4) = csv;
        }
        f32x16 acc[7];
#pragma unroll
        for (int a = 0; a < 7; ++a)
#pragma unroll
            for (int i = 0; i < 16; ++i) acc[a][i] = 0.f;
        bf16x8 xf[3], wf[3][7];
        float sa[2] = {0.f, 0.f}, sb[2] = {0.f, 0.f};
#pragma unroll
        for (int g = 0; g < 42; ++g) {
            const int s = g >> 2, j = g & 3;
            if (g < 40) {
                if (j == 0) {
                    asm volatile("s_waitcnt lgkmcnt(0)" ::: "memory");
                    __builtin_amdgcn_s_barrier();
                    asm volatile("" ::: "memory");
                }
                xf[g % 3] = *(const bf16x8*)(lds + lb[s % 3] + tb * 4096 + j * 1024);
#pragma unroll
                for (int i = 0; i < 7; ++i) wf[g % 3][i] = *(const bf16x8*)(lds + lb[s % 3] + (4 + i) * 4096 + j * 1024);
            }
            if (g >= 2) {
                const int c = (g - 2) % 3;
#pragma unroll
                for (int i = 0; i < 7; ++i)
                    acc[i] = __builtin_amdgcn_mfma_f32_32x32x16_bf16(wf[c][i], xf[c], acc[i], 0, 0, 0);
                if (((g - 2) & 3) >= 2) {
                    u32x4 xx = __builtin_bit_cast(u32x4, xf[c]);
#pragma unroll
                    for (int e = 0; e < 4; ++e) {
                        float lo = lo2f(xx[e]), hi = hi2f(xx[e]);
                        sa[e & 1] += lo + hi;
                        sb[e & 1] = __builtin_fmaf(lo, lo, __builtin_fmaf(hi, hi, sb[e & 1]));
                    }
                }
#pragma unroll
                for (int i = 0; i < 7; ++i) {
                    __builtin_amdgcn_sched_group_barrier(0x8, 1, 0);
                    __builtin_amdgcn_sched_group_barrier(0x100, 1, 0);
                    __builtin_amdgcn_sched_group_barrier(0x2, 4, 0);
                }
            }
            __builtin_amdgcn_sched_barrier(0);
        }
#pragma unroll
        for (int i = 4; i < 7; ++i)
#pragma unroll
            for (int r = 0; r < 4; ++r) {
                f32x4 t = {acc[i][4 * r], acc[i][4 * r + 1], acc[i][4 * r + 2], acc[i][4 * r + 3]};
                *(f32x4*)(lds + G1_STAGE + (tb * 12 + (i - 4) * 4 + r) * 1024 + lane * 16) = t;
            }
        { f32x2 st = {sa[0] + sa[1], sb[0] + sb[1]}; *(f32x2*)(lds + G1_ST_OFF + 2048 + (tb * 64 + lane) * 8) = st; }
        __syncthreads();
        float mb, rinv;
        g1_stats(lds, tb, tk, mb, rinv);
#pragma unroll
        for (int i = 0; i < 4; ++i) {
            int u = ubase + i;
            if (u < NUNIT) g1_epi_unit(acc[i], lds + G1_CS_OFF + i * 128 + hh * 16, mb, rinv, hr, token, u, hh);
        }
    }
}

constexpr int PROW = 336;
constexpr int PTILE = 64 * PROW;
constexpr int PD2 = 3;

__device__ __forceinline__ void g2_phase(const bool MID, char* lds, const u16* __restrict__ hp, const u16* __restrict__ W2p,
                                         const u16* __restrict__ xresF, const u16* __restrict__ yF,
                                         u16* __restrict__ xout, unsigned long long* gran, u32* wcnt) {
    const int tid = threadIdx.x, lane = tid & 63;
    const int w = __builtin_amdgcn_readfirstlane(tid >> 6);
    const int xcd = blockIdx.x & 7, jj = blockIdx.x >> 3;
    const int rt = 8 * (xcd >> 1) + (jj & 7), ct = 4 * (xcd & 1) + (jj >> 3);

    const rsrc_t hr = mk_rsrc(hp, HP_BYTES);
    const u32 hoff = (u32)(((4 * rt) * KS2 * 64 + lane) * 16);
    const bf16x8* wbp = (const bf16x8*)W2p + (long)(5 * ct) * KS2 * 64 + lane;
    const int nsteps = (w < 6) ? 7 : 6;
#define LDH(m, s) __builtin_bit_cast(bf16x8, ld16_dev(hr, hoff + (u32)(((m) * KS2 + (s)) * 1024)))

    bf16x8 hf[PD2][4], wf[PD2][5];
#pragma unroll
    for (int i = 0; i < PD2; ++i) {
        int s = w + 8 * i;
#pragma unroll
        for (int n = 0; n < 5; ++n) wf[i][n] = wbp[(long)(n * KS2 + s) * 64];
    }
    const int tok = lane;
    const int token = 64 * rt + tok;
    const rsrc_t xr = mk_rsrc(xresF, X_BYTES);
    u32x4 res[2], yv[2];
#pragma unroll
    for (int it = 0; it < 2; ++it) {
        int cc = w + 8 * it;
        if (cc < 10) {
            long idx = xf_idx(token, 80 * ct + 8 * cc);
            res[it] = ld16_dev(xr, (u32)idx * 2);
            if (MID) yv[it] = *(const u32x4*)(yF + idx);
        }
    }
    wait_count(wcnt, 16);
#pragma unroll
    for (int i = 0; i < PD2; ++i) {
        int s = w + 8 * i;
#pragma unroll
        for (int m = 0; m < 4; ++m) hf[i][m] = LDH(m, s);
    }
    f32x4 acc[5][4];
#pragma unroll
    for (int n = 0; n < 5; ++n)
#pragma unroll
        for (int m = 0; m < 4; ++m) acc[n][m] = (f32x4){0.f, 0.f, 0.f, 0.f};
#pragma unroll
    for (int i = 0; i < 7; ++i) {
        if (i < nsteps) {
#pragma unroll
            for (int n = 0; n < 5; ++n)
#pragma unroll
                for (int m = 0; m < 4; ++m)
                    acc[n][m] = __builtin_amdgcn_mfma_f32_16x16x32_bf16(wf[i % PD2][n], hf[i % PD2][m], acc[n][m], 0, 0, 0);
            if (i + PD2 < nsteps) {
                int s = w + 8 * (i + PD2);
#pragma unroll
                for (int m = 0; m < 4; ++m) hf[i % PD2][m] = LDH(m, s);
#pragma unroll
                for (int n = 0; n < 5; ++n) wf[i % PD2][n] = wbp[(long)(n * KS2 + s) * 64];
            }
        }
    }
#undef LDH
    const int tl = lane & 15, q = lane >> 4;
    char* slot = lds + (w & 3) * PTILE + tl * PROW + q * 16;
    if (w >= 4) {
#pragma unroll
        for (int n = 0; n < 5; ++n)
#pragma unroll
            for (int m = 0; m < 4; ++m) *(f32x4*)(slot + m * 16 * PROW + n * 64) = acc[n][m];
    }
    __syncthreads();
    if (w < 4) {
#pragma unroll
        for (int n = 0; n < 5; ++n)
#pragma unroll
            for (int m = 0; m < 4; ++m) {
                f32x4 o = *(const f32x4*)(slot + m * 16 * PROW + n * 64);
                *(f32x4*)(slot + m * 16 * PROW + n * 64) = o + acc[n][m];
            }
    }
    __syncthreads();
    u32x4 x2[2];
#pragma unroll
    for (int it = 0; it < 2; ++it) {
        int cc = w + 8 * it;
        if (cc < 10) {
            const char* p = lds + tok * PROW + cc * 32;
            f32x4 s0 = *(const f32x4*)(p), s1 = *(const f32x4*)(p + 16);
#pragma unroll
            for (int k = 1; k < 4; ++k) {
                s0 += *(const f32x4*)(p + k * PTILE);
                s1 += *(const f32x4*)(p + k * PTILE + 16);
            }
            u32x4 rs = res[it];
            x2[it][0] = pack2(lo2f(rs[0]) + rbf(s0[0]), hi2f(rs[0]) + rbf(s0[1]));
            x2[it][1] = pack2(lo2f(rs[1]) + rbf(s0[2]), hi2f(rs[1]) + rbf(s0[3]));
            x2[it][2] = pack2(lo2f(rs[2]) + rbf(s1[0]), hi2f(rs[2]) + rbf(s1[1]));
            x2[it][3] = pack2(lo2f(rs[3]) + rbf(s1[2]), hi2f(rs[3]) + rbf(s1[3]));
            if (!MID) *(u32x4*)(xout + (long)token * D_ + 80 * ct + 8 * cc) = x2[it];
        }
    }
    if (MID) {
        __syncthreads();
        float* red = (float*)lds;
        float* red2 = (float*)(lds + 8192);
#pragma unroll
        for (int it = 0; it < 2; ++it) {
            int cc = w + 8 * it;
            if (cc < 10) {
                float a = 0.f, b = 0.f;
#pragma unroll
                for (int e = 0; e < 4; ++e) {
                    float lo = lo2f(x2[it][e]), hi = hi2f(x2[it][e]);
                    a += lo + hi;
                    b = __builtin_fmaf(lo, lo, __builtin_fmaf(hi, hi, b));
                }
                red[cc * 64 + tok] = a;
                red[640 + cc * 64 + tok] = b;
            }
        }
        __syncthreads();
        if (w < 2) {
            float t = 0.f;
#pragma unroll
            for (int c = 0; c < 10; ++c) t += red[w * 640 + c * 64 + tok];
            unsigned long long g = ((unsigned long long)GTAG << 32) | (unsigned long long)__float_as_uint(t);
            __hip_atomic_store((gu64*)(gran + ((rt * 8 + ct) * 2 + w) * 64 + tok), g, __ATOMIC_RELAXED, __HIP_MEMORY_SCOPE_AGENT);
        }
        {
            unsigned long long g0, g1;
            gu64* p0 = (gu64*)(gran + ((rt * 8 + w) * 2 + 0) * 64 + tok);
            gu64* p1 = (gu64*)(gran + ((rt * 8 + w) * 2 + 1) * 64 + tok);
            for (unsigned spins = 0; spins < (1u << 22); ++spins) {
                g0 = __hip_atomic_load(p0, __ATOMIC_RELAXED, __HIP_MEMORY_SCOPE_AGENT);
                g1 = __hip_atomic_load(p1, __ATOMIC_RELAXED, __HIP_MEMORY_SCOPE_AGENT);
                bool ok = ((u32)(g0 >> 32) == GTAG) && ((u32)(g1 >> 32) == GTAG);
                if (__all(ok)) break;
                __builtin_amdgcn_s_sleep(1);
            }
            red2[w * 64 + tok] = __uint_as_float((u32)g0);
            red2[512 + w * 64 + tok] = __uint_as_float((u32)g1);
        }
        __syncthreads();
        float a = 0.f, b = 0.f;
#pragma unroll
        for (int c = 0; c < 8; ++c) { a += red2[c * 64 + tok]; b += red2[512 + c * 64 + tok]; }
        const float mean = a * (1.0f / 640.0f);
        float var = b * (1.0f / 640.0f) - mean * mean;
        var = var > 0.f ? var : 0.f;
        float mb, rinv;
        ln_finish(mean, var, mb, rinv);
        const rsrc_t xo = mk_rsrc(xout, X_BYTES);
#pragma unroll
        for (int it = 0; it < 2; ++it) {
            int cc = w + 8 * it;
            if (cc < 10) {
                u32x4 o;
#pragma unroll
                for (int e = 0; e < 4; ++e) {
                    float xa = lo2f(x2[it][e]), xb = hi2f(x2[it][e]);
                    float qa = rbf(rbf(xa - mb) * rinv), qb = rbf(rbf(xb - mb) * rinv);
                    float ga = rbf(qa * lo2f(yv[it][e])), gb = rbf(qb * hi2f(yv[it][e]));
                    o[e] = pack2(xa + ga, xb + gb);
                }
                st16_dev(xo, (u32)xf_idx(token, 80 * ct + 8 * cc) * 2, o);
            }
        }
    }
}

__global__ __launch_bounds__(512, 2) void ffn_kernel(const u16* __restrict__ xaF, const u16* __restrict__ W1p,
                                                     const float* __restrict__ cs, u16* __restrict__ hpA,
                                                     u16* __restrict__ hpB, const u16* __restrict__ W2p,
                                                     const u16* __restrict__ yF, u16* __restrict__ xbF,
                                                     u16* __restrict__ out, unsigned long long* gran, u32* cnt) {
    __shared__ __attribute__((aligned(16))) char lds[FFN_LDS];
    const int xcd = blockIdx.x & 7, jj = blockIdx.x >> 3;
    const int rt1 = 8 * (xcd >> 2) + (jj & 7);
    const int rg2 = (8 * (xcd >> 1) + (jj & 7)) >> 1;
#pragma unroll 1
    for (int layer = 0; layer < 2; ++layer) {
        g1_phase(layer != 0, lds, layer ? xbF : xaF, W1p, cs, layer ? hpB : hpA, cnt + (1 * 16 + rt1) * 32);
        signal_count(cnt + (2 * layer * 16 + rt1) * 32);
        g2_phase(layer == 0, lds, layer ? hpB : hpA, W2p, layer ? xbF : xaF, yF, layer ? out : xbF, gran,
                 cnt + (2 * layer * 16 + rg2) * 32);
        if (layer == 0) signal_count(cnt + (1 * 16 + rg2) * 32);
    }
}

extern "C" void kernel_launch(void* const* d_in, const int* in_sizes, int n_in,
                              void* d_out, int out_size, void* d_ws, size_t ws_size,
                              hipStream_t stream) {
    const u16* q = (const u16*)d_in[0];
    const u16* k = (const u16*)d_in[1];
    const u16* v = (const u16*)d_in[2];
    const u16* sk = (const u16*)d_in[3];
    const u16* ssmk = (const u16*)d_in[4];
    const u16* ssmD = (const u16*)d_in[5];
    const u16* wg = (const u16*)d_in[7];
    const u16* wu = (const u16*)d_in[8];
    const u16* wd = (const u16*)d_in[9];
    u16* out = (u16*)d_out;

    char* ws = (char*)d_ws;
    constexpr size_t SZ_X = (size_t)S_ * D_ * 2;
    size_t off = 0;
    unsigned long long* gran = (unsigned long long*)(ws + off); off += (size_t)GRAN_WORDS * 8;
    u32* cnt = (u32*)(ws + off); off += 8192;
    u16* W1p = (u16*)(ws + off); off += (size_t)NUNIT * KS1 * 1024;
    u16* kvR = (u16*)(ws + off); off += SZ_X;
    u16* yF = (u16*)(ws + off); off += SZ_X;
    u16* xaF = (u16*)(ws + off); off += SZ_X;
    u16* xbF = (u16*)(ws + off); off += SZ_X;
    u16* qF = (u16*)(ws + off); off += SZ_X;
    u16* W2p = (u16*)(ws + off); off += (size_t)40 * KS2 * 1024;
    u16* hpA = (u16*)(ws + off); off += (size_t)128 * KS2 * 1024;
    u16* hpB = (u16*)(ws + off); off += (size_t)128 * KS2 * 1024;
    f32x2* qs = (f32x2*)(ws + off); off += (size_t)S_ * 8;
    float* cs = (float*)(ws + off); off += (size_t)NUNIT * 32 * 4;

    prep_kernel<<<320 + 512, 256, 0, stream>>>(k, v, sk, kvR, q, qF, qs, (u32x4*)gran);
    conv_kernel<<<NCONV + NREP1 + NREP2 + NCS, 256, 0, stream>>>(kvR, ssmk, ssmD, yF, qF, qs, xaF, wg, wu, wd, W1p, W2p, cs);
    ffn_kernel<<<256, 512, 0, stream>>>(xaF, W1p, cs, hpA, hpB, W2p, yF, xbF, out, gran, cnt);
}
```

```cpp
#include <hip/hip_runtime.h>
#include <hip/hip_bf16.h>
#include <stdint.h>

typedef unsigned short u16;
typedef unsigned int u32;
typedef __attribute__((ext_vector_type(8))) short bf16x8;
typedef __attribute__((ext_vector_type(4))) float f32x4;
typedef __attribute__((ext_vector_type(16))) float f32x16;
typedef __attribute__((ext_vector_type(4))) u32 u32x4;
typedef __attribute__((ext_vector_type(2))) u32 u32x2;
typedef __attribute__((ext_vector_type(2))) float f32x2;
typedef __attribute__((ext_vector_type(2))) __bf16 bf16x2v;

constexpr int S_ = 2048, D_ = 640, F_ = 1728;
constexpr int NUNIT = 108;
constexpr int KS1 = 40;
constexpr int KS2 = 54;

__device__ __forceinline__ float lo2f(u32 v) { return __uint_as_float(v << 16); }
__device__ __forceinline__ float hi2f(u32 v) { return __uint_as_float(v & 0xffff0000u); }
__device__ __forceinline__ u32 pack2(float a, float b) {
    f32x2 f = {a, b};
    bf16x2v r = __builtin_convertvector(f, bf16x2v);
    return __builtin_bit_cast(u32, r);
}
__device__ __forceinline__ float rbf(float a) {
    __bf16 b = (__bf16)a;
    return __uint_as_float(((u32)__builtin_bit_cast(u16, b)) << 16);
}
__device__ __forceinline__ u16 f2bf(float a) {
    __bf16 b = (__bf16)a;
    return __builtin_bit_cast(u16, b);
}

__device__ __forceinline__ float rowsum16(float x) {
    x += __builtin_bit_cast(float, __builtin_amdgcn_update_dpp(0, __builtin_bit_cast(int, x), 0x128, 0xf, 0xf, false));
    x += __builtin_bit_cast(float, __builtin_amdgcn_update_dpp(0, __builtin_bit_cast(int, x), 0x124, 0xf, 0xf, false));
    x += __builtin_bit_cast(float, __builtin_amdgcn_update_dpp(0, __builtin_bit_cast(int, x), 0x122, 0xf, 0xf, false));
    x += __builtin_bit_cast(float, __builtin_amdgcn_update_dpp(0, __builtin_bit_cast(int, x), 0x121, 0xf, 0xf, false));
    return x;
}

__device__ __forceinline__ float wavesum(float x) {
    x = rowsum16(x);
    x += __shfl_xor(x, 16);
    x += __shfl_xor(x, 32);
    return x;
}
__device__ __forceinline__ long xf_idx(int token, int d) {
    return ((long)((token >> 5) * 80 + (d >> 3)) * 32 + (token & 31)) * 8 + (d & 7);
}
__device__ __forceinline__ float dot2bf(u32 a, u32 b, float c) {
    return __builtin_amdgcn_fdot2_f32_bf16(__builtin_bit_cast(bf16x2v, a), __builtin_bit_cast(bf16x2v, b), c, false);
}
__device__ __forceinline__ void ln_finish(float mean, float var, float& mb, float& rinv) {
    mb = rbf(mean);
    float den = rbf(__builtin_sqrtf(rbf(rbf(var) + 1e-5f)));
    rinv = 1.0f / den;
}
constexpr u32 GTAG = 0x13572468u;
constexpr int GRAN_WORDS = 32 * 8 * 2 * 64;

constexpr int NREP1 = 40;
constexpr int NREP2 = 27;
constexpr int NCS = 27;
__global__ __launch_bounds__(256) void prep_kernel(const u16* __restrict__ k, const u16* __restrict__ v,
                                                   const u16* __restrict__ sk, u16* __restrict__ kvR,
                                                   const u16* __restrict__ q, u16* __restrict__ qF,
                                                   f32x2* __restrict__ qs, u32x4* __restrict__ gran) {
    __shared__ __attribute__((aligned(16))) u32 T[64 * 32];
    if (blockIdx.x >= 320) {
        const int b = blockIdx.x - 320;
        if (b < 66) { u32x4 z = {0u, 0u, 0u, 0u}; gran[b * 256 + threadIdx.x] = z; }
        const int lane = threadIdx.x & 63;
        const int row = b * 4 + (threadIdx.x >> 6);
        const u32* xs = (const u32*)(q + (long)row * D_);
        u32 xv[5];
#pragma unroll
        for (int m = 0; m < 5; ++m) xv[m] = xs[lane + 64 * m];
        float sum = 0.f;
#pragma unroll
        for (int m = 0; m < 5; ++m) sum += lo2f(xv[m]) + hi2f(xv[m]);
        sum = wavesum(sum);
        float mean = sum * (1.0f / 640.0f);
        float sq = 0.f;
#pragma unroll
        for (int m = 0; m < 5; ++m) {
            float a = lo2f(xv[m]) - mean, c = hi2f(xv[m]) - mean;
            sq += a * a + c * c;
        }
        sq = wavesum(sq);
        float mb, rinv;
        ln_finish(mean, sq * (1.0f / 640.0f), mb, rinv);
        if (lane == 0) { f32x2 o = {mb, rinv}; qs[row] = o; }
#pragma unroll
        for (int m = 0; m < 5; ++m) *(u32*)(qF + xf_idx(row, 2 * (lane + 64 * m))) = xv[m];
        return;
    }
    const int st = blockIdx.x / 10, dt = blockIdx.x % 10;
    const int s0 = st * 64, d0 = dt * 64;
    const int t = threadIdx.x, dc = t & 7, rp = t >> 3;
    const int s = s0 + 2 * rp, d = d0 + 8 * dc;
    u32x4 kr[5];
#pragma unroll
    for (int i = 0; i < 5; ++i) {
        int row = s - 3 + i;
        u32x4 z = {0u, 0u, 0u, 0u};
        kr[i] = (row >= 0) ? *(const u32x4*)(k + (long)row * D_ + d) : z;
    }
    u32x4 v0 = *(const u32x4*)(v + (long)s * D_ + d);
    u32x4 v1 = *(const u32x4*)(v + (long)(s + 1) * D_ + d);
    u32x4 skv[4];
#pragma unroll
    for (int i = 0; i < 4; ++i) skv[i] = *(const u32x4*)(sk + (long)d * 4 + 8 * i);
    const int p = 31 - rp;
#pragma unroll
    for (int e = 0; e < 8; ++e) {
        u32 w01 = skv[e >> 1][(e & 1) * 2 + 0], w23 = skv[e >> 1][(e & 1) * 2 + 1];
        float c0 = lo2f(w01), c1 = hi2f(w01), c2 = lo2f(w23), c3 = hi2f(w23);
        float kk[5];
#pragma unroll
        for (int i = 0; i < 5; ++i) {
            u32 wv = kr[i][e >> 1];
            kk[i] = (e & 1) ? hi2f(wv) : lo2f(wv);
        }
        float va = (e & 1) ? hi2f(v0[e >> 1]) : lo2f(v0[e >> 1]);
        float vb = (e & 1) ? hi2f(v1[e >> 1]) : lo2f(v1[e >> 1]);
        float a = rbf(kk[0] * c3);
        a = rbf(a + rbf(kk[1] * c2));
        a = rbf(a + rbf(kk[2] * c1));
        a = rbf(a + rbf(kk[3] * c0));
        float b = rbf(kk[1] * c3);
        b = rbf(b + rbf(kk[2] * c2));
        b = rbf(b + rbf(kk[3] * c1));
        b = rbf(b + rbf(kk[4] * c0));
        u32 pk = pack2(b * vb, a * va);
        int dl = 8 * dc + e;
        T[dl * 32 + ((((p >> 2) ^ dc)) << 2) + (p & 3)] = pk;
    }
    __syncthreads();
#pragma unroll
    for (int it = 0; it < 2; ++it) {
        int idx = t + 256 * it;
        int dl = idx >> 3, c = idx & 7;
        u32x4 o = *(const u32x4*)&T[dl * 32 + ((c ^ (dl >> 3)) << 2)];
        *(u32x4*)(kvR + (long)(d0 + dl) * S_ + (1984 - s0) + 8 * c) = o;
    }
}

constexpr int HC_STRIDE = 4416;
constexpr int KV_BYTES = 47 * 144;
constexpr int WAVE_LDS = 4 * HC_STRIDE + KV_BYTES;
constexpr int YBUF_OFF = 4 * WAVE_LDS;
constexpr int YCH = 32 * 136;
constexpr int CONV_LDS = YBUF_OFF + 4 * YCH;
constexpr int NCONV = 160;

constexpr int PART_OFF = CONV_LDS;
constexpr int CONV_LDS2 = PART_OFF + 4 * 4096;

template <int K0, int K1, int NT>
__device__ __forceinline__ void conv_steps(const char* lds, u32 aoffA, u32 aoffB, u32 aoffC, u32 aoffD, u32 boff,
                                           f32x4 (&acc)[4]) {
    bf16x8 A[2 * (K1 - K0) + 4], B[K1 - K0];
#define LOADA(i) do { \
        u32x2 l0 = *(const u32x2*)(lds + (((i) & 1) ? aoffC : aoffA) + 64 * ((i) >> 1)); \
        u32x2 l1 = *(const u32x2*)(lds + (((i) & 1) ? aoffD : aoffB) + 64 * ((i) >> 1)); \
        u32x4 t4 = {l0[0], l0[1], l1[0], l1[1]}; \
        A[(i) - 2 * K0] = __builtin_bit_cast(bf16x8, t4); } while (0)
#define LOADB(ks) do { \
        B[(ks) - K0] = *(const bf16x8*)(lds + boff + 16 * (9 * ((ks) >> 1) + 4 * ((ks) & 1)) - NT * 16 * 9 * 16); } while (0)
#pragma unroll
    for (int i = 2 * K0; i < 2 * K0 + 6; ++i) LOADA(i);
    LOADB(K0);
    LOADB(K0 + 1);
    __builtin_amdgcn_sched_barrier(0);
#pragma unroll
    for (int ks = K0; ks < K1; ++ks) {
        if (ks + 2 < K1) {
            LOADA(2 * ks + 6);
            LOADA(2 * ks + 7);
            LOADB(ks + 2);
        }
        __builtin_amdgcn_sched_barrier(0);
#pragma unroll
        for (int m = 0; m < 4; ++m)
            acc[m] = __builtin_amdgcn_mfma_f32_16x16x32_bf16(A[2 * (ks - K0) + m], B[ks - K0], acc[m], 0, 0, 0);
        __builtin_amdgcn_sched_barrier(0);
    }
#undef LOADA
#undef LOADB
}

__global__ __launch_bounds__(512, 2) void conv_kernel(const u16* __restrict__ kvR, const u16* __restrict__ ssmk,
                                                      const u16* __restrict__ ssmD, u16* __restrict__ yF,
                                                      const u16* __restrict__ qF,
                                                      const f32x2* __restrict__ qs, u16* __restrict__ xaF,
                                                      const u16* __restrict__ wg, const u16* __restrict__ wu,
                                                      const u16* __restrict__ wd, u16* __restrict__ W1p,
                                                      u16* __restrict__ W2p, float* __restrict__ cs) {
    __shared__ __attribute__((aligned(16))) char lds[CONV_LDS2];
    const int tid = threadIdx.x, lane = tid & 63;
    const int w8 = __builtin_amdgcn_readfirstlane(tid >> 6);
    const int bid = blockIdx.x;
    if (bid < NCONV) {
        const int w = w8 & 3, half = w8 >> 2;
        const int ch = bid * 4 + w;
        char* wl = lds + w * WAVE_LDS;
        char* kvl = wl + 4 * HC_STRIDE;
        const u32x4* hsrc = (const u32x4*)(ssmk + (long)ch * S_);
        const u32x4* ksrc = (const u32x4*)(kvR + (long)ch * S_);
        u32x4 kvv[4], ha[3], hb[3];
#pragma unroll
        for (int mm = 0; mm < 3; ++mm) {
            int c = lane - 8 + 64 * (3 * half + mm);
            int c0 = c < 0 ? 0 : (c > 255 ? 255 : c);
            int c1 = c + 1 < 0 ? 0 : (c + 1 > 255 ? 255 : c + 1);
            ha[mm] = hsrc[c0];
            hb[mm] = hsrc[c1];
        }
        if (half) {
#pragma unroll
            for (int m = 0; m < 4; ++m) kvv[m] = ksrc[lane + 64 * m];
        }
        u32x2 qv[4];
        f32x2 qst[4];
#pragma unroll
        for (int it = 0; it < 4; ++it) {
            int s = tid + 512 * it;
            qv[it] = *(const u32x2*)(qF + xf_idx(s, bid * 4));
            qst[it] = qs[s];
        }
#pragma unroll
        for (int mm = 0; mm < 3; ++mm) {
            int c = lane - 8 + 64 * (3 * half + mm);
            u32x4 z = {0u, 0u, 0u, 0u};
            u32x4 a = (c >= 0) ? ha[mm] : z;
            u32x4 b = (c + 1 >= 0 && c + 1 < 256) ? hb[mm] : z;
            u32 ww[8] = {a[0], a[1], a[2], a[3], b[0], b[1], b[2], b[3]};
            u32x4 o0, o1, o2, o3;
#pragma unroll
            for (int i = 0; i < 4; ++i) {
                o0[i] = __builtin_amdgcn_alignbit(ww[i + 1], ww[i], 16);
                o1[i] = ww[i + 1];
                o2[i] = __builtin_amdgcn_alignbit(ww[i + 2], ww[i + 1], 16);
                o3[i] = ww[i + 2];
            }
            if (c < 256) {
                char* dst = wl + (c + 8) * 16;
                *(u32x4*)(dst + 0 * HC_STRIDE) = o0;
                *(u32x4*)(dst + 1 * HC_STRIDE) = o1;
                *(u32x4*)(dst + 2 * HC_STRIDE) = o2;
                *(u32x4*)(dst + 3 * HC_STRIDE) = o3;
            }
        }
        if (half) {
#pragma unroll
            for (int m = 0; m < 4; ++m) {
                int c = lane + 64 * m;
                *(u32x4*)(kvl + (c >> 3) * 144 + (c & 7) * 16) = kvv[m];
            }
        } else {
            u32x4 z = {0u, 0u, 0u, 0u};
#pragma unroll
            for (int m = 0; m < 3; ++m) {
                int c = lane + 64 * m;
                if (c < 135) *(u32x4*)(kvl + 32 * 144 + c * 16) = z;
            }
        }
        __syncthreads();

        const int r = lane & 15, kq = lane >> 4;
        u32 aoffA = (u32)(w * WAVE_LDS + (r & 3) * HC_STRIDE + 2 * ((r & 12) + 8 * kq));
        u32 aoffB = aoffA + 8, aoffC = aoffA + 32, aoffD = aoffA + 40;
        asm volatile("" : "+v"(aoffB));
        asm volatile("" : "+v"(aoffC));
        asm volatile("" : "+v"(aoffD));
        const u32 boff = (u32)(w * WAVE_LDS + 4 * HC_STRIDE + 16 * (9 * (31 - r) + kq));

        f32x4 acc[4];
#pragma unroll
        for (int m = 0; m < 4; ++m) acc[m] = (f32x4){0.f, 0.f, 0.f, 0.f};
        char* part = lds + PART_OFF + w * 4096 + lane * 16;
        if (half == 0) {
            conv_steps<0, 48, 1>(lds, aoffA, aoffB, aoffC, aoffD, boff, acc);
        } else {
            conv_steps<48, 64, 1>(lds, aoffA, aoffB, aoffC, aoffD, boff, acc);
#pragma unroll
            for (int m = 0; m < 4; ++m) { *(f32x4*)(part + m * 1024) = acc[m]; acc[m] = (f32x4){0.f, 0.f, 0.f, 0.f}; }
            conv_steps<0, 32, 0>(lds, aoffA, aoffB, aoffC, aoffD, boff, acc);
        }
        __syncthreads();
        if (half == 0) {
#pragma unroll
            for (int m = 0; m < 4; ++m) acc[m] += *(const f32x4*)(part + m * 1024);
        }
        const float Dv = lo2f((u32)ssmD[ch]);
        char* ybuf = lds + YBUF_OFF;
        {
            const int a = (half ? 0 : 16) + r;
#pragma unroll
            for (int m = 0; m < 4; ++m) {
                int ib = 60 - 16 * m - 4 * kq;
                u32x2 kk = *(const u32x2*)(kvl + (31 - a) * 144 + ib * 2);
                float kvf[4] = {hi2f(kk[1]), lo2f(kk[1]), hi2f(kk[0]), lo2f(kk[0])};
                float yo[4];
#pragma unroll
                for (int j = 0; j < 4; ++j) yo[j] = rbf(acc[m][j]) + rbf(kvf[j] * Dv);
                u32x2 o = {pack2(yo[0], yo[1]), pack2(yo[2], yo[3])};
                *(u32x2*)(ybuf + w * YCH + a * 136 + (16 * m + 4 * kq) * 2) = o;
            }
        }
        __syncthreads();
#pragma unroll
        for (int it = 0; it < 4; ++it) {
            int s = tid + 512 * it;
            const char* yp = ybuf + (s >> 6) * 136 + (s & 63) * 2;
            u32x2 yo;
            yo[0] = (u32)*(const u16*)(yp) | ((u32)*(const u16*)(yp + YCH) << 16);
            yo[1] = (u32)*(const u16*)(yp + 2 * YCH) | ((u32)*(const u16*)(yp + 3 * YCH) << 16);
            float mb = qst[it][0], rinv = qst[it][1];
            u32x2 xo;
#pragma unroll
            for (int j = 0; j < 2; ++j) {
                float a = lo2f(qv[it][j]), b = hi2f(qv[it][j]);
                float qa = rbf(rbf(a - mb) * rinv), qb = rbf(rbf(b - mb) * rinv);
                float ga = rbf(qa * lo2f(yo[j])), gb = rbf(qb * hi2f(yo[j]));
                xo[j] = pack2(a + ga, b + gb);
            }
            long idx = xf_idx(s, bid * 4);
            *(u32x2*)(yF + idx) = yo;
            *(u32x2*)(xaF + idx) = xo;
        }
    } else if (bid < NCONV + NREP1) {
        const int ks = bid - NCONV;
        u16* T = (u16*)lds;
        constexpr int TS = 1736;
        u32x4 t[14];
#pragma unroll
        for (int it = 0; it < 14; ++it) {
            int i = tid + 512 * it;
            if (i > 6911) i = 6911;
            int mat = i / 3456, rem = i % 3456, kr = rem / 216, cc = rem % 216;
            t[it] = *(const u32x4*)((mat ? wu : wg) + (long)(16 * ks + kr) * F_ + 8 * cc);
        }
#pragma unroll
        for (int it = 0; it < 14; ++it) {
            int i = tid + 512 * it;
            if (i > 6911) i = 6911;
            int mat = i / 3456, rem = i % 3456, kr = rem / 216, cc = rem % 216;
            *(u32x4*)(T + (mat * 16 + kr) * TS + 8 * cc) = t[it];
        }
        __syncthreads();
#pragma unroll 7
        for (int it = 0; it < 14; ++it) {
            int i = tid + 512 * it;
            if (i < 6912) {
                int uu = i >> 6, l = i & 63;
                int ii = l & 31, hh = l >> 5;
                int mat = ii >> 4, col = 16 * uu + (ii & 15);
                const u16* tp = T + (mat * 16 + 8 * hh) * TS + col;
                u32x4 o;
#pragma unroll
                for (int j = 0; j < 4; ++j) o[j] = (u32)tp[(2 * j) * TS] | ((u32)tp[(2 * j + 1) * TS] << 16);
                *(u32x4*)(W1p + ((long)(uu * KS1 + ks) * 64 + l) * 8) = o;
            }
        }
    } else if (bid < NCONV + NREP1 + NREP2) {
        const int s0 = 2 * (bid - NCONV - NREP1);
        u16* T = (u16*)lds;
        constexpr int TS = 648;
        u32x4 t[10];
#pragma unroll
        for (int it = 0; it < 10; ++it) {
            int i = tid + 512 * it;
            int fr = i / 80, cc = i % 80;
            t[it] = *(const u32x4*)(wd + (long)(32 * s0 + fr) * D_ + 8 * cc);
        }
#pragma unroll
        for (int it = 0; it < 10; ++it) {
            int i = tid + 512 * it;
            int fr = i / 80, cc = i % 80;
            *(u32x4*)(T + fr * TS + 8 * cc) = t[it];
        }
        __syncthreads();
#pragma unroll 5
        for (int it = 0; it < 10; ++it) {
            int i = tid + 512 * it;
            int sl = i / 2560, rem = i % 2560;
            int nt = rem >> 6, l = rem & 63;
            int idx = l & 15, kq = l >> 4;
            int fb = 32 * sl + 16 * (kq >> 1) + 4 * (kq & 1);
            const u16* tp = T + 16 * nt + idx;
            u32x4 o;
            o[0] = (u32)tp[(fb + 0) * TS] | ((u32)tp[(fb + 1) * TS] << 16);
            o[1] = (u32)tp[(fb + 2) * TS] | ((u32)tp[(fb + 3) * TS] << 16);
            o[2] = (u32)tp[(fb + 8) * TS] | ((u32)tp[(fb + 9) * TS] << 16);
            o[3] = (u32)tp[(fb + 10) * TS] | ((u32)tp[(fb + 11) * TS] << 16);
            *(u32x4*)(W2p + ((long)(nt * KS2 + s0 + sl) * 64 + l) * 8) = o;
        }
    } else if (bid < NCONV + NREP1 + NREP2 + NCS) {
        const int cb = bid - NCONV - NREP1 - NREP2;
        const int cp = tid & 31, kg = tid >> 5;
        float a0 = 0.f, a1 = 0.f, b0 = 0.f, b1 = 0.f;
#pragma unroll 20
        for (int i = 0; i < 40; ++i) {
            u32 wv = *(const u32*)(wg + (long)(kg * 40 + i) * F_ + cb * 64 + 2 * cp);
            u32 uv = *(const u32*)(wu + (long)(kg * 40 + i) * F_ + cb * 64 + 2 * cp);
            a0 += lo2f(wv); a1 += hi2f(wv);
            b0 += lo2f(uv); b1 += hi2f(uv);
        }
        float* T = (float*)lds;
        T[kg * 64 + 2 * cp] = a0;
        T[kg * 64 + 2 * cp + 1] = a1;
        T[1024 + kg * 64 + 2 * cp] = b0;
        T[1024 + kg * 64 + 2 * cp + 1] = b1;
        __syncthreads();
        if (tid < 128) {
            int mat = tid >> 6, c = tid & 63;
            float t = 0.f;
#pragma unroll
            for (int g = 0; g < 16; ++g) t += T[mat * 1024 + g * 64 + c];
            int f = cb * 64 + c;
            cs[(f >> 4) * 32 + mat * 16 + (f & 15)] = t;
        }
    }
}

constexpr int G1_STAGE = 44 * 1024;
constexpr int G1_CS_OFF = 3 * G1_STAGE;

typedef __attribute__((address_space(1))) unsigned long long gu64;
typedef __attribute__((address_space(1))) unsigned int gu32;
typedef __amdgpu_buffer_rsrc_t rsrc_t;
__device__ __forceinline__ rsrc_t mk_rsrc(const void* p, u32 bytes) {
    return __builtin_amdgcn_make_buffer_rsrc((void*)p, 0, bytes, 0x00020000);
}
__device__ __forceinline__ void st16_dev(rsrc_t r, u32 off, u32x4 v) { __builtin_amdgcn_raw_buffer_store_b128(v, r, off, 0, 16); }
__device__ __forceinline__ u32x4 ld16_dev(rsrc_t r, u32 off) { return __builtin_amdgcn_raw_buffer_load_b128(r, off, 0, 16); }

__device__ __forceinline__ void wait_count(u32* cnt, u32 target) {
    for (unsigned spins = 0; spins < (1u << 22); ++spins) {
        u32 v = __hip_atomic_load((gu32*)cnt, __ATOMIC_RELAXED, __HIP_MEMORY_SCOPE_AGENT);
        if (v >= target) break;
        __builtin_amdgcn_s_sleep(2);
    }
    __builtin_amdgcn_fence(__ATOMIC_ACQUIRE, "wavefront");
    asm volatile("" ::: "memory");
}
__device__ __forceinline__ void signal_count(u32* cnt) {
    asm volatile("s_waitcnt vmcnt(0)" ::: "memory");
    __syncthreads();
    if (threadIdx.x == 0) __hip_atomic_fetch_add((gu32*)cnt, 1u, __ATOMIC_RELAXED, __HIP_MEMORY_SCOPE_AGENT);
}

__device__ __forceinline__ void g1_epi_unit(const f32x16& a, const char* cp, float mb, float rinv, rsrc_t hr,
                                            int token, int u, int hh) {
    f32x4 c0 = *(const f32x4*)(cp), c1 = *(const f32x4*)(cp + 32);
    f32x4 c2 = *(const f32x4*)(cp + 64), c3 = *(const f32x4*)(cp + 96);
    float hv[8];
#pragma unroll
    for (int e = 0; e < 8; ++e) {
        float cg = (e < 4) ? c0[e & 3] : c1[e & 3];
        float cu = (e < 4) ? c2[e & 3] : c3[e & 3];
        u32 gu = pack2((a[e] - mb * cg) * rinv, (a[e + 8] - mb * cu) * rinv);
        float g = lo2f(gu), uu = hi2f(gu);
        float sg = rbf(__builtin_amdgcn_rcpf(1.0f + __expf(-g)));
        hv[e] = sg * uu;
    }
    u32x4 o = {pack2(hv[0], hv[1]), pack2(hv[2], hv[3]), pack2(hv[4], hv[5]), pack2(hv[6], hv[7])};
    u32 idx = (u32)(((token >> 4) * KS2 + (u >> 1)) * 64 + (token & 15) + 16 * (2 * (u & 1) + hh));
    st16_dev(hr, idx * 16, o);
}

constexpr int G1_ST_OFF = G1_CS_OFF + 1024;
constexpr int FFN_LDS = 3 * G1_STAGE + 1024 + 4096;
constexpr u32 HP_BYTES = 128u * KS2 * 1024u;
constexpr u32 X_BYTES = (u32)S_ * D_ * 2u;
__device__ __forceinline__ void g1_stats(const char* lds, int tb, int tk, float& mb, float& rinv) {
    f32x2 a = *(const f32x2*)(lds + G1_ST_OFF + (tb * 64 + tk) * 8);
    f32x2 b = *(const f32x2*)(lds + G1_ST_OFF + (tb * 64 + tk + 32) * 8);
    f32x2 c = *(const f32x2*)(lds + G1_ST_OFF + 2048 + (tb * 64 + tk) * 8);
    f32x2 d = *(const f32x2*)(lds + G1_ST_OFF + 2048 + (tb * 64 + tk + 32) * 8);
    float s1 = (a[0] + b[0]) + (c[0] + d[0]), s2 = (a[1] + b[1]) + (c[1] + d[1]);
    const float mean = s1 * (1.0f / 640.0f);
    float var = s2 * (1.0f / 640.0f) - mean * mean;
    var = var > 0.f ? var : 0.f;
    ln_finish(mean, var, mb, rinv);
}

template <bool WAIT>
__device__ __forceinline__ void g1_phase(char* lds, const u16* __restrict__ xF, const u16* __restrict__ W1p,
                                         const float* __restrict__ cs, u16* __restrict__ hp, u32* wcnt) {
    const int tid = threadIdx.x, lane = tid & 63;
    const int w = __builtin_amdgcn_readfirstlane(tid >> 6);
    const int xcd = blockIdx.x & 7, jj = blockIdx.x >> 3;
    const int rt = 8 * (xcd >> 2) + (jj & 7), ct = 4 * (xcd & 3) + (jj >> 3);
    const int ubase = ct * 7;
    const int tb = w & 3;
    const int tk = lane & 31, hh = lane >> 5;
    const int token = 128 * rt + 32 * tb + tk;
    const rsrc_t hr = mk_rsrc(hp, HP_BYTES);
    u32 lb[3];
#pragma unroll
    for (int b = 0; b < 3; ++b) { lb[b] = (u32)(b * G1_STAGE + lane * 16); asm volatile("" : "+v"(lb[b])); }

    if (w >= 4) {
        const int lw = w - 4;
        const char* wbase = (const char*)W1p;
        const char* xbase = (const char*)xF;
        u32 srcw[7];
        const u32 srcx = (u32)((((4 * rt + lw) * KS1) * 64 + lane) * 16);
#pragma unroll
        for (int i = 0; i < 7; ++i) {
            int q = lw * 7 + i;
            int uu = ubase + (q >> 2);
            if (uu > NUNIT - 1) uu = NUNIT - 1;
            srcw[i] = (u32)(((uu * KS1 + (q & 3)) * 64 + lane) * 16);
        }
#define G1_ISSUE_W(s) do { \
        _Pragma("unroll") for (int i = 0; i < 7; ++i) \
            __builtin_amdgcn_global_load_lds((const u32*)(wbase + (srcw[i] + (u32)((s) * 4096))), \
                (u32*)(lds + ((s) % 3) * G1_STAGE + (16 + lw * 7 + i) * 1024 + lane * 16), 16, 0, 0); \
        } while (0)
#define G1_ISSUE_X(s) do { \
        _Pragma("unroll") for (int i = 0; i < 4; ++i) \
            __builtin_amdgcn_global_load_lds((const u32*)(xbase + (srcx + (u32)((s) * 4096 + i * 1024))), \
                (u32*)(lds + ((s) % 3) * G1_STAGE + (lw * 4 + i) * 1024 + lane * 16), 16, 0, WAIT ? 16 : 0); \
        } while (0)
        G1_ISSUE_W(0);
        G1_ISSUE_W(1);
        if (WAIT) wait_count(wcnt, 16);
        G1_ISSUE_X(0);
        G1_ISSUE_X(1);
        float sa[4] = {0.f, 0.f, 0.f, 0.f}, sb[4] = {0.f, 0.f, 0.f, 0.f};
#pragma unroll
        for (int s = 0; s < 10; ++s) {
            if (s == 0) asm volatile("s_waitcnt vmcnt(4) lgkmcnt(0)" ::: "memory");
            else if (s < 9) asm volatile("s_waitcnt vmcnt(11) lgkmcnt(0)" ::: "memory");
            else asm volatile("s_waitcnt vmcnt(0) lgkmcnt(0)" ::: "memory");
            __builtin_amdgcn_s_barrier();
            asm volatile("" ::: "memory");
            u32x4 xx[2];
#pragma unroll
            for (int j = 0; j < 2; ++j) xx[j] = *(const u32x4*)(lds + lb[s % 3] + tb * 4096 + j * 1024);
            if (s + 2 < 10) { G1_ISSUE_W(s + 2); G1_ISSUE_X(s + 2); }
#pragma unroll
            for (int j = 0; j < 2; ++j)
#pragma unroll
                for (int e = 0; e < 4; ++e) {
                    float lo = lo2f(xx[j][e]), hi = hi2f(xx[j][e]);
                    sa[e] += lo + hi;
                    sb[e] = __builtin_fmaf(lo, lo, __builtin_fmaf(hi, hi, sb[e]));
                }
        }
#undef G1_ISSUE_W
#undef G1_ISSUE_X
        float s1 = (sa[0] + sa[1]) + (sa[2] + sa[3]), s2 = (sb[0] + sb[1]) + (sb[2] + sb[3]);
        { f32x2 st = {s1, s2}; *(f32x2*)(lds + G1_ST_OFF + (tb * 64 + lane) * 8) = st; }
        __syncthreads();
        float mb, rinv;
        g1_stats(lds, tb, tk, mb, rinv);
#pragma unroll
        for (int i = 4; i < 7; ++i) {
            int u = ubase + i;
            if (u < NUNIT) {
                f32x16 a;
#pragma unroll
                for (int r = 0; r < 4; ++r) {
                    f32x4 t = *(const f32x4*)(lds + G1_STAGE + (tb * 12 + (i - 4) * 4 + r) * 1024 + lane * 16);
                    a[4 * r] = t[0]; a[4 * r + 1] = t[1]; a[4 * r + 2] = t[2]; a[4 * r + 3] = t[3];
                }
                g1_epi_unit(a, lds + G1_CS_OFF + i * 128 + hh * 16, mb, rinv, hr, token, u, hh);
            }
        }
    } else {
        if (lane < 56) {
            int e = ubase * 32 + w * 56 + lane;
            float csv = (e < NUNIT * 32) ? cs[e] : 0.f;
            *(float*)(lds + G1_CS_OFF + (w * 56 + lane) * 4) = csv;
        }
        f32x16 acc[7];
#pragma unroll
        for (int a = 0; a < 7; ++a)
#pragma unroll
            for (int i = 0; i < 16; ++i) acc[a][i] = 0.f;
        bf16x8 xf[3], wf[3][7];
        float sa[2] = {0.f, 0.f}, sb[2] = {0.f, 0.f};
#pragma unroll
        for (int g = 0; g < 42; ++g) {
            const int s = g >> 2, j = g & 3;
            if (g < 40) {
                if (j == 0) {
                    asm volatile("s_waitcnt lgkmcnt(0)" ::: "memory");
                    __builtin_amdgcn_s_barrier();
                    asm volatile("" ::: "memory");
                }
                xf[g % 3] = *(const bf16x8*)(lds + lb[s % 3] + tb * 4096 + j * 1024);
#pragma unroll
                for (int i = 0; i < 7; ++i) wf[g % 3][i] = *(const bf16x8*)(lds + lb[s % 3] + (4 + i) * 4096 + j * 1024);
            }
            if (g >= 2) {
                const int c = (g - 2) % 3;
#pragma unroll
                for (int i = 0; i < 7; ++i)
                    acc[i] = __builtin_amdgcn_mfma_f32_32x32x16_bf16(wf[c][i], xf[c], acc[i], 0, 0, 0);
                if (((g - 2) & 3) >= 2) {
                    u32x4 xx = __builtin_bit_cast(u32x4, xf[c]);
#pragma unroll
                    for (int e = 0; e < 4; ++e) {
                        float lo = lo2f(xx[e]), hi = hi2f(xx[e]);
                        sa[e & 1] += lo + hi;
                        sb[e & 1] = __builtin_fmaf(lo, lo, __builtin_fmaf(hi, hi, sb[e & 1]));
                    }
                }
#pragma unroll
                for (int i = 0; i < 7; ++i) {
                    __builtin_amdgcn_sched_group_barrier(0x8, 1, 0);
                    __builtin_amdgcn_sched_group_barrier(0x100, 1, 0);
                    __builtin_amdgcn_sched_group_barrier(0x2, 4, 0);
                }
            }
            __builtin_amdgcn_sched_barrier(0);
        }
#pragma unroll
        for (int i = 4; i < 7; ++i)
#pragma unroll
            for (int r = 0; r < 4; ++r) {
                f32x4 t = {acc[i][4 * r], acc[i][4 * r + 1], acc[i][4 * r + 2], acc[i][4 * r + 3]};
                *(f32x4*)(lds + G1_STAGE + (tb * 12 + (i - 4) * 4 + r) * 1024 + lane * 16) = t;
            }
        { f32x2 st = {sa[0] + sa[1], sb[0] + sb[1]}; *(f32x2*)(lds + G1_ST_OFF + 2048 + (tb * 64 + lane) * 8) = st; }
        __syncthreads();
        float mb, rinv;
        g1_stats(lds, tb, tk, mb, rinv);
#pragma unroll
        for (int i = 0; i < 4; ++i) {
            int u = ubase + i;
            if (u < NUNIT) g1_epi_unit(acc[i], lds + G1_CS_OFF + i * 128 + hh * 16, mb, rinv, hr, token, u, hh);
        }
    }
}

constexpr int PROW = 336;
constexpr int PTILE = 64 * PROW;
constexpr int PD2 = 3;

template <bool MID>
__device__ __forceinline__ void g2_phase(char* lds, const u16* __restrict__ hp, const u16* __restrict__ W2p,
                                         const u16* __restrict__ xresF, const u16* __restrict__ yF,
                                         u16* __restrict__ xout, unsigned long long* gran, u32* wcnt) {
    const int tid = threadIdx.x, lane = tid & 63;
    const int w = __builtin_amdgcn_readfirstlane(tid >> 6);
    const int xcd = blockIdx.x & 7, jj = blockIdx.x >> 3;
    const int rt = 8 * (xcd >> 1) + (jj & 7), ct = 4 * (xcd & 1) + (jj >> 3);

    const rsrc_t hr = mk_rsrc(hp, HP_BYTES);
    const u32 hoff = (u32)(((4 * rt) * KS2 * 64 + lane) * 16);
    const bf16x8* wbp = (const bf16x8*)W2p + (long)(5 * ct) * KS2 * 64 + lane;
    const int nsteps = (w < 6) ? 7 : 6;
#define LDH(m, s) __builtin_bit_cast(bf16x8, ld16_dev(hr, hoff + (u32)(((m) * KS2 + (s)) * 1024)))

    bf16x8 hf[PD2][4], wf[PD2][5];
#pragma unroll
    for (int i = 0; i < PD2; ++i) {
        int s = w + 8 * i;
#pragma unroll
        for (int n = 0; n < 5; ++n) wf[i][n] = wbp[(long)(n * KS2 + s) * 64];
    }
    const int tok = lane;
    const int token = 64 * rt + tok;
    const rsrc_t xr = mk_rsrc(xresF, X_BYTES);
    u32x4 res[2], yv[2];
#pragma unroll
    for (int it = 0; it < 2; ++it) {
        int cc = w + 8 * it;
        if (cc < 10) {
            long idx = xf_idx(token, 80 * ct + 8 * cc);
            res[it] = ld16_dev(xr, (u32)idx * 2);
            if (MID) yv[it] = *(const u32x4*)(yF + idx);
        }
    }
    wait_count(wcnt, 16);
#pragma unroll
    for (int i = 0; i < PD2; ++i) {
        int s = w + 8 * i;
#pragma unroll
        for (int m = 0; m < 4; ++m) hf[i][m] = LDH(m, s);
    }
    f32x4 acc[5][4];
#pragma unroll
    for (int n = 0; n < 5; ++n)
#pragma unroll
        for (int m = 0; m < 4; ++m) acc[n][m] = (f32x4){0.f, 0.f, 0.f, 0.f};
#pragma unroll
    for (int i = 0; i < 7; ++i) {
        if (i < nsteps) {
#pragma unroll
            for (int n = 0; n < 5; ++n)
#pragma unroll
                for (int m = 0; m < 4; ++m)
                    acc[n][m] = __builtin_amdgcn_mfma_f32_16x16x32_bf16(wf[i % PD2][n], hf[i % PD2][m], acc[n][m], 0, 0, 0);
            if (i + PD2 < nsteps) {
                int s = w + 8 * (i + PD2);
#pragma unroll
                for (int m = 0; m < 4; ++m) hf[i % PD2][m] = LDH(m, s);
#pragma unroll
                for (int n = 0; n < 5; ++n) wf[i % PD2][n] = wbp[(long)(n * KS2 + s) * 64];
            }
        }
    }
#undef LDH
    const int tl = lane & 15, q = lane >> 4;
    char* slot = lds + (w & 3) * PTILE + tl * PROW + q * 16;
    if (w >= 4) {
#pragma unroll
        for (int n = 0; n < 5; ++n)
#pragma unroll
            for (int m = 0; m < 4; ++m) *(f32x4*)(slot + m * 16 * PROW + n * 64) = acc[n][m];
    }
    __syncthreads();
    if (w < 4) {
#pragma unroll
        for (int n = 0; n < 5; ++n)
#pragma unroll
            for (int m = 0; m < 4; ++m) {
                f32x4 o = *(const f32x4*)(slot + m * 16 * PROW + n * 64);
                *(f32x4*)(slot + m * 16 * PROW + n * 64) = o + acc[n][m];
            }
    }
    __syncthreads();
    u32x4 x2[2];
#pragma unroll
    for (int it = 0; it < 2; ++it) {
        int cc = w + 8 * it;
        if (cc < 10) {
            const char* p = lds + tok * PROW + cc * 32;
            f32x4 s0 = *(const f32x4*)(p), s1 = *(const f32x4*)(p + 16);
#pragma unroll
            for (int k = 1; k < 4; ++k) {
                s0 += *(const f32x4*)(p + k * PTILE);
                s1 += *(const f32x4*)(p + k * PTILE + 16);
            }
            u32x4 rs = res[it];
            x2[it][0] = pack2(lo2f(rs[0]) + rbf(s0[0]), hi2f(rs[0]) + rbf(s0[1]));
            x2[it][1] = pack2(lo2f(rs[1]) + rbf(s0[2]), hi2f(rs[1]) + rbf(s0[3]));
            x2[it][2] = pack2(lo2f(rs[2]) + rbf(s1[0]), hi2f(rs[2]) + rbf(s1[1]));
            x2[it][3] = pack2(lo2f(rs[3]) + rbf(s1[2]), hi2f(rs[3]) + rbf(s1[3]));
            if (!MID) *(u32x4*)(xout + (long)token * D_ + 80 * ct + 8 * cc) = x2[it];
        }
    }
    if (MID) {
        __syncthreads();
        float* red = (float*)lds;
        float* red2 = (float*)(lds + 8192);
#pragma unroll
        for (int it = 0; it < 2; ++it) {
            int cc = w + 8 * it;
            if (cc < 10) {
                float a = 0.f, b = 0.f;
#pragma unroll
                for (int e = 0; e < 4; ++e) {
                    float lo = lo2f(x2[it][e]), hi = hi2f(x2[it][e]);
                    a += lo + hi;
                    b = __builtin_fmaf(lo, lo, __builtin_fmaf(hi, hi, b));
                }
                red[cc * 64 + tok] = a;
                red[640 + cc * 64 + tok] = b;
            }
        }
        __syncthreads();
        if (w < 2) {
            float t = 0.f;
#pragma unroll
            for (int c = 0; c < 10; ++c) t += red[w * 640 + c * 64 + tok];
            unsigned long long g = ((unsigned long long)GTAG << 32) | (unsigned long long)__float_as_uint(t);
            __hip_atomic_store((gu64*)(gran + ((rt * 8 + ct) * 2 + w) * 64 + tok), g, __ATOMIC_RELAXED, __HIP_MEMORY_SCOPE_AGENT);
        }
        {
            unsigned long long g0, g1;
            gu64* p0 = (gu64*)(gran + ((rt * 8 + w) * 2 + 0) * 64 + tok);
            gu64* p1 = (gu64*)(gran + ((rt * 8 + w) * 2 + 1) * 64 + tok);
            for (unsigned spins = 0; spins < (1u << 22); ++spins) {
                g0 = __hip_atomic_load(p0, __ATOMIC_RELAXED, __HIP_MEMORY_SCOPE_AGENT);
                g1 = __hip_atomic_load(p1, __ATOMIC_RELAXED, __HIP_MEMORY_SCOPE_AGENT);
                bool ok = ((u32)(g0 >> 32) == GTAG) && ((u32)(g1 >> 32) == GTAG);
                if (__all(ok)) break;
                __builtin_amdgcn_s_sleep(1);
            }
            red2[w * 64 + tok] = __uint_as_float((u32)g0);
            red2[512 + w * 64 + tok] = __uint_as_float((u32)g1);
        }
        __syncthreads();
        float a = 0.f, b = 0.f;
#pragma unroll
        for (int c = 0; c < 8; ++c) { a += red2[c * 64 + tok]; b += red2[512 + c * 64 + tok]; }
        const float mean = a * (1.0f / 640.0f);
        float var = b * (1.0f / 640.0f) - mean * mean;
        var = var > 0.f ? var : 0.f;
        float mb, rinv;
        ln_finish(mean, var, mb, rinv);
        const rsrc_t xo = mk_rsrc(xout, X_BYTES);
#pragma unroll
        for (int it = 0; it < 2; ++it) {
            int cc = w + 8 * it;
            if (cc < 10) {
                u32x4 o;
#pragma unroll
                for (int e = 0; e < 4; ++e) {
                    float xa = lo2f(x2[it][e]), xb = hi2f(x2[it][e]);
                    float qa = rbf(rbf(xa - mb) * rinv), qb = rbf(rbf(xb - mb) * rinv);
                    float ga = rbf(qa * lo2f(yv[it][e])), gb = rbf(qb * hi2f(yv[it][e]));
                    o[e] = pack2(xa + ga, xb + gb);
                }
                st16_dev(xo, (u32)xf_idx(token, 80 * ct + 8 * cc) * 2, o);
            }
        }
    }
}

__global__ __launch_bounds__(512, 2) void ffn_kernel(const u16* __restrict__ xaF, const u16* __restrict__ W1p,
                                                     const float* __restrict__ cs, u16* __restrict__ hpA,
                                                     u16* __restrict__ hpB, const u16* __restrict__ W2p,
                                                     const u16* __restrict__ yF, u16* __restrict__ xbF,
                                                     u16* __restrict__ out, unsigned long long* gran, u32* cnt) {
    __shared__ __attribute__((aligned(16))) char lds[FFN_LDS];
    const int xcd = blockIdx.x & 7, jj = blockIdx.x >> 3;
    const int rt1 = 8 * (xcd >> 2) + (jj & 7);
    const int rg2 = (8 * (xcd >> 1) + (jj & 7)) >> 1;
    g1_phase<false>(lds, xaF, W1p, cs, hpA, nullptr);
    signal_count(cnt + (0 * 16 + rt1) * 32);
    g2_phase<true>(lds, hpA, W2p, xaF, yF, xbF, gran, cnt + (0 * 16 + rg2) * 32);
    signal_count(cnt + (1 * 16 + rg2) * 32);
    g1_phase<true>(lds, xbF, W1p, cs, hpB, cnt + (1 * 16 + rt1) * 32);
    signal_count(cnt + (2 * 16 + rt1) * 32);
    g2_phase<false>(lds, hpB, W2p, xbF, yF, out, gran, cnt + (2 * 16 + rg2) * 32);
}

extern "C" void kernel_launch(void* const* d_in, const int* in_sizes, int n_in,
                              void* d_out, int out_size, void* d_ws, size_t ws_size,
                              hipStream_t stream) {
    const u16* q = (const u16*)d_in[0];
    const u16* k = (const u16*)d_in[1];
    const u16* v = (const u16*)d_in[2];
    const u16* sk = (const u16*)d_in[3];
    const u16* ssmk = (const u16*)d_in[4];
    const u16* ssmD = (const u16*)d_in[5];
    const u16* wg = (const u16*)d_in[7];
    const u16* wu = (const u16*)d_in[8];
    const u16* wd = (const u16*)d_in[9];
    u16* out = (u16*)d_out;

    char* ws = (char*)d_ws;
    constexpr size_t SZ_X = (size_t)S_ * D_ * 2;
    size_t off = 0;
    unsigned long long* gran = (unsigned long long*)(ws + off); off += (size_t)GRAN_WORDS * 8;
    u32* cnt = (u32*)(ws + off); off += 8192;
    u16* W1p = (u16*)(ws + off); off += (size_t)NUNIT * KS1 * 1024;
    u16* kvR = (u16*)(ws + off); off += SZ_X;
    u16* yF = (u16*)(ws + off); off += SZ_X;
    u16* xaF = (u16*)(ws + off); off += SZ_X;
    u16* xbF = (u16*)(ws + off); off += SZ_X;
    u16* qF = (u16*)(ws + off); off += SZ_X;
    u16* W2p = (u16*)(ws + off); off += (size_t)40 * KS2 * 1024;
    u16* hpA = (u16*)(ws + off); off += (size_t)128 * KS2 * 1024;
    u16* hpB = (u16*)(ws + off); off += (size_t)128 * KS2 * 1024;
    f32x2* qs = (f32x2*)(ws + off); off += (size_t)S_ * 8;
    float* cs = (float*)(ws + off); off += (size_t)NUNIT * 32 * 4;

    prep_kernel<<<320 + 512, 256, 0, stream>>>(k, v, sk, kvR, q, qF, qs, (u32x4*)gran);
    conv_kernel<<<NCONV + NREP1 + NREP2 + NCS, 512, 0, stream>>>(kvR, ssmk, ssmD, yF, qF, qs, xaF, wg, wu, wd, W1p, W2p, cs);
    ffn_kernel<<<256, 512, 0, stream>>>(xaF, W1p, cs, hpA, hpB, W2p, yF, xbF, out, gran, cnt);
}
```

```cpp
#include <hip/hip_runtime.h>
#include <hip/hip_bf16.h>
#include <stdint.h>

typedef unsigned short u16;
typedef unsigned int u32;
typedef __attribute__((ext_vector_type(8))) short bf16x8;
typedef __attribute__((ext_vector_type(4))) float f32x4;
typedef __attribute__((ext_vector_type(16))) float f32x16;
typedef __attribute__((ext_vector_type(4))) u32 u32x4;
typedef __attribute__((ext_vector_type(2))) u32 u32x2;
typedef __attribute__((ext_vector_type(2))) float f32x2;
typedef __attribute__((ext_vector_type(2))) __bf16 bf16x2v;

constexpr int S_ = 2048, D_ = 640, F_ = 1728;
constexpr int NUNIT = 108;
constexpr int KS1 = 40;
constexpr int KS2 = 54;

__device__ __forceinline__ float lo2f(u32 v) { return __uint_as_float(v << 16); }
__device__ __forceinline__ float hi2f(u32 v) { return __uint_as_float(v & 0xffff0000u); }
__device__ __forceinline__ u32 pack2(float a, float b) {
    f32x2 f = {a, b};
    bf16x2v r = __builtin_convertvector(f, bf16x2v);
    return __builtin_bit_cast(u32, r);
}
__device__ __forceinline__ float rbf(float a) {
    __bf16 b = (__bf16)a;
    return __uint_as_float(((u32)__builtin_bit_cast(u16, b)) << 16);
}
__device__ __forceinline__ u16 f2bf(float a) {
    __bf16 b = (__bf16)a;
    return __builtin_bit_cast(u16, b);
}

__device__ __forceinline__ float rowsum16(float x) {
    x += __builtin_bit_cast(float, __builtin_amdgcn_update_dpp(0, __builtin_bit_cast(int, x), 0x128, 0xf, 0xf, false));
    x += __builtin_bit_cast(float, __builtin_amdgcn_update_dpp(0, __builtin_bit_cast(int, x), 0x124, 0xf, 0xf, false));
    x += __builtin_bit_cast(float, __builtin_amdgcn_update_dpp(0, __builtin_bit_cast(int, x), 0x122, 0xf, 0xf, false));
    x += __builtin_bit_cast(float, __builtin_amdgcn_update_dpp(0, __builtin_bit_cast(int, x), 0x121, 0xf, 0xf, false));
    return x;
}

__device__ __forceinline__ float wavesum(float x) {
    x = rowsum16(x);
    x += __shfl_xor(x, 16);
    x += __shfl_xor(x, 32);
    return x;
}
__device__ __forceinline__ long xf_idx(int token, int d) {
    return ((long)((token >> 5) * 80 + (d >> 3)) * 32 + (token & 31)) * 8 + (d & 7);
}
__device__ __forceinline__ float dot2bf(u32 a, u32 b, float c) {
    return __builtin_amdgcn_fdot2_f32_bf16(__builtin_bit_cast(bf16x2v, a), __builtin_bit_cast(bf16x2v, b), c, false);
}
__device__ __forceinline__ void ln_finish(float mean, float var, float& mb, float& rinv) {
    mb = rbf(mean);
    float den = rbf(__builtin_sqrtf(rbf(rbf(var) + 1e-5f)));
    rinv = 1.0f / den;
}
constexpr u32 GTAG = 0x13572468u;
constexpr int GRAN_WORDS = 32 * 8 * 2 * 64;

constexpr int NREP1 = 40;
constexpr int NREP2 = 27;
constexpr int NCS = 27;
__global__ __launch_bounds__(256) void prep_kernel(const u16* __restrict__ k, const u16* __restrict__ v,
                                                   const u16* __restrict__ sk, u16* __restrict__ kvR,
                                                   const u16* __restrict__ q, u16* __restrict__ qF,
                                                   f32x2* __restrict__ qs, u32x4* __restrict__ gran) {
    __shared__ __attribute__((aligned(16))) u32 T[64 * 32];
    if (blockIdx.x >= 320) {
        const int b = blockIdx.x - 320;
        if (b < 66) { u32x4 z = {0u, 0u, 0u, 0u}; gran[b * 256 + threadIdx.x] = z; }
        const int lane = threadIdx.x & 63;
        const int row = b * 4 + (threadIdx.x >> 6);
        const u32* xs = (const u32*)(q + (long)row * D_);
        u32 xv[5];
#pragma unroll
        for (int m = 0; m < 5; ++m) xv[m] = xs[lane + 64 * m];
        float sum = 0.f;
#pragma unroll
        for (int m = 0; m < 5; ++m) sum += lo2f(xv[m]) + hi2f(xv[m]);
        sum = wavesum(sum);
        float mean = sum * (1.0f / 640.0f);
        float sq = 0.f;
#pragma unroll
        for (int m = 0; m < 5; ++m) {
            float a = lo2f(xv[m]) - mean, c = hi2f(xv[m]) - mean;
            sq += a * a + c * c;
        }
        sq = wavesum(sq);
        float mb, rinv;
        ln_finish(mean, sq * (1.0f / 640.0f), mb, rinv);
        if (lane == 0) { f32x2 o = {mb, rinv}; qs[row] = o; }
#pragma unroll
        for (int m = 0; m < 5; ++m) *(u32*)(qF + xf_idx(row, 2 * (lane + 64 * m))) = xv[m];
        return;
    }
    const int st = blockIdx.x / 10, dt = blockIdx.x % 10;
    const int s0 = st * 64, d0 = dt * 64;
    const int t = threadIdx.x, dc = t & 7, rp = t >> 3;
    const int s = s0 + 2 * rp, d = d0 + 8 * dc;
    u32x4 kr[5];
#pragma unroll
    for (int i = 0; i < 5; ++i) {
        int row = s - 3 + i;
        u32x4 z = {0u, 0u, 0u, 0u};
        kr[i] = (row >= 0) ? *(const u32x4*)(k + (long)row * D_ + d) : z;
    }
    u32x4 v0 = *(const u32x4*)(v + (long)s * D_ + d);
    u32x4 v1 = *(const u32x4*)(v + (long)(s + 1) * D_ + d);
    u32x4 skv[4];
#pragma unroll
    for (int i = 0; i < 4; ++i) skv[i] = *(const u32x4*)(sk + (long)d * 4 + 8 * i);
    const int p = 31 - rp;
#pragma unroll
    for (int e = 0; e < 8; ++e) {
        u32 w01 = skv[e >> 1][(e & 1) * 2 + 0], w23 = skv[e >> 1][(e & 1) * 2 + 1];
        float c0 = lo2f(w01), c1 = hi2f(w01), c2 = lo2f(w23), c3 = hi2f(w23);
        float kk[5];
#pragma unroll
        for (int i = 0; i < 5; ++i) {
            u32 wv = kr[i][e >> 1];
            kk[i] = (e & 1) ? hi2f(wv) : lo2f(wv);
        }
        float va = (e & 1) ? hi2f(v0[e >> 1]) : lo2f(v0[e >> 1]);
        float vb = (e & 1) ? hi2f(v1[e >> 1]) : lo2f(v1[e >> 1]);
        float a = rbf(kk[0] * c3);
        a = rbf(a + rbf(kk[1] * c2));
        a = rbf(a + rbf(kk[2] * c1));
        a = rbf(a + rbf(kk[3] * c0));
        float b = rbf(kk[1] * c3);
        b = rbf(b + rbf(kk[2] * c2));
        b = rbf(b + rbf(kk[3] * c1));
        b = rbf(b + rbf(kk[4] * c0));
        u32 pk = pack2(b * vb, a * va);
        int dl = 8 * dc + e;
        T[dl * 32 + ((((p >> 2) ^ dc)) << 2) + (p & 3)] = pk;
    }
    __syncthreads();
#pragma unroll
    for (int it = 0; it < 2; ++it) {
        int idx = t + 256 * it;
        int dl = idx >> 3, c = idx & 7;
        u32x4 o = *(const u32x4*)&T[dl * 32 + ((c ^ (dl >> 3)) << 2)];
        *(u32x4*)(kvR + (long)(d0 + dl) * S_ + (1984 - s0) + 8 * c) = o;
    }
}

constexpr int HC_STRIDE = 4416;
constexpr int KV_BYTES = 47 * 144;
constexpr int WAVE_LDS = 4 * HC_STRIDE + KV_BYTES;
constexpr int YBUF_OFF = 4 * WAVE_LDS;
constexpr int YCH = 32 * 136;
constexpr int CONV_LDS = YBUF_OFF + 4 * YCH;
constexpr int NCONV = 160;

__global__ __launch_bounds__(256, 1) void conv_kernel(const u16* __restrict__ kvR, const u16* __restrict__ ssmk,
                                                      const u16* __restrict__ ssmD, u16* __restrict__ yF,
                                                      const u16* __restrict__ qF,
                                                      const f32x2* __restrict__ qs, u16* __restrict__ xaF,
                                                      const u16* __restrict__ wg, const u16* __restrict__ wu,
                                                      const u16* __restrict__ wd, u16* __restrict__ W1p,
                                                      u16* __restrict__ W2p, float* __restrict__ cs) {
    __shared__ __attribute__((aligned(16))) char lds[CONV_LDS];
    const int tid = threadIdx.x, lane = tid & 63, w = tid >> 6;
    const int bid = blockIdx.x;
    if (bid < NCONV) {
        const int ch = bid * 4 + w;
        char* wl = lds + w * WAVE_LDS;
        char* kvl = wl + 4 * HC_STRIDE;
        const u32x4* hsrc = (const u32x4*)(ssmk + (long)ch * S_);
        const u32x4* ksrc = (const u32x4*)(kvR + (long)ch * S_);
        u32x4 kvv[4], ha[5], hb[5];
#pragma unroll
        for (int m = 0; m < 5; ++m) {
            int c = lane - 8 + 64 * m;
            int c0 = c < 0 ? 0 : (c > 255 ? 255 : c);
            int c1 = c + 1 < 0 ? 0 : (c + 1 > 255 ? 255 : c + 1);
            ha[m] = hsrc[c0];
            hb[m] = hsrc[c1];
        }
#pragma unroll
        for (int m = 0; m < 4; ++m) kvv[m] = ksrc[lane + 64 * m];
#pragma unroll
        for (int m = 0; m < 5; ++m) {
            int c = lane - 8 + 64 * m;
            u32x4 z = {0u, 0u, 0u, 0u};
            u32x4 a = (c >= 0) ? ha[m] : z;
            u32x4 b = (c + 1 >= 0 && c + 1 < 256) ? hb[m] : z;
            u32 ww[8] = {a[0], a[1], a[2], a[3], b[0], b[1], b[2], b[3]};
            u32x4 o0, o1, o2, o3;
#pragma unroll
            for (int i = 0; i < 4; ++i) {
                o0[i] = __builtin_amdgcn_alignbit(ww[i + 1], ww[i], 16);
                o1[i] = ww[i + 1];
                o2[i] = __builtin_amdgcn_alignbit(ww[i + 2], ww[i + 1], 16);
                o3[i] = ww[i + 2];
            }
            if (m < 4 || c < 256) {
                char* dst = wl + (c + 8) * 16;
                *(u32x4*)(dst + 0 * HC_STRIDE) = o0;
                *(u32x4*)(dst + 1 * HC_STRIDE) = o1;
                *(u32x4*)(dst + 2 * HC_STRIDE) = o2;
                *(u32x4*)(dst + 3 * HC_STRIDE) = o3;
            }
        }
#pragma unroll
        for (int m = 0; m < 4; ++m) {
            int c = lane + 64 * m;
            *(u32x4*)(kvl + (c >> 3) * 144 + (c & 7) * 16) = kvv[m];
        }
        {
            u32x4 z = {0u, 0u, 0u, 0u};
#pragma unroll
            for (int m = 0; m < 3; ++m) {
                int c = lane + 64 * m;
                if (c < 135) *(u32x4*)(kvl + 32 * 144 + c * 16) = z;
            }
        }
        __builtin_amdgcn_s_waitcnt(0xC07F);
        __builtin_amdgcn_wave_barrier();
        u32x2 qv[8];
        f32x2 qst[8];
#pragma unroll
        for (int it = 0; it < 8; ++it) {
            int s = tid + 256 * it;
            qv[it] = *(const u32x2*)(qF + xf_idx(s, bid * 4));
            qst[it] = qs[s];
        }

        const int r = lane & 15, kq = lane >> 4;
        u32 aoffA = (u32)(w * WAVE_LDS + (r & 3) * HC_STRIDE + 2 * ((r & 12) + 8 * kq));
        u32 aoffB = aoffA + 8, aoffC = aoffA + 32, aoffD = aoffA + 40;
        asm volatile("" : "+v"(aoffB));
        asm volatile("" : "+v"(aoffC));
        asm volatile("" : "+v"(aoffD));
        const u32 boff = (u32)(w * WAVE_LDS + 4 * HC_STRIDE + 16 * (9 * (31 - r) + kq));

        f32x4 acc[2][4];
#pragma unroll
        for (int n = 0; n < 2; ++n)
#pragma unroll
            for (int m = 0; m < 4; ++m) acc[n][m] = (f32x4){0.f, 0.f, 0.f, 0.f};

        bf16x8 A[136], B0[32], B1[64];
#define LOADA(i) do { \
            u32x2 l0 = *(const u32x2*)(lds + (((i) & 1) ? aoffC : aoffA) + 64 * ((i) >> 1)); \
            u32x2 l1 = *(const u32x2*)(lds + (((i) & 1) ? aoffD : aoffB) + 64 * ((i) >> 1)); \
            u32x4 t4 = {l0[0], l0[1], l1[0], l1[1]}; \
            A[i] = __builtin_bit_cast(bf16x8, t4); } while (0)
#define KSO(ks) (16 * (9 * ((ks) >> 1) + 4 * ((ks) & 1)))
#define LOADB(ks) do { \
            B1[ks] = *(const bf16x8*)(lds + boff + KSO(ks) - 16 * 9 * 16); \
            if ((ks) < 32) B0[(ks) < 32 ? (ks) : 0] = *(const bf16x8*)(lds + boff + KSO(ks)); } while (0)
#pragma unroll
        for (int i = 0; i < 6; ++i) LOADA(i);
        LOADB(0);
        LOADB(1);
        __builtin_amdgcn_sched_barrier(0);
#pragma unroll
        for (int ks = 0; ks < 64; ++ks) {
            if (ks + 2 < 64) {
                LOADA(2 * ks + 6);
                LOADA(2 * ks + 7);
                LOADB(ks + 2);
            }
            __builtin_amdgcn_sched_barrier(0);
#pragma unroll
            for (int m = 0; m < 4; ++m)
                acc[1][m] = __builtin_amdgcn_mfma_f32_16x16x32_bf16(A[2 * ks + m], B1[ks], acc[1][m], 0, 0, 0);
            if (ks < 32) {
#pragma unroll
                for (int m = 0; m < 4; ++m)
                    acc[0][m] = __builtin_amdgcn_mfma_f32_16x16x32_bf16(A[2 * ks + m], B0[ks < 32 ? ks : 0], acc[0][m], 0, 0, 0);
            }
            __builtin_amdgcn_sched_barrier(0);
        }
#undef LOADA
#undef LOADB
#undef KSO
        const float Dv = lo2f((u32)ssmD[ch]);
        char* ybuf = lds + YBUF_OFF;
#pragma unroll
        for (int n = 0; n < 2; ++n)
#pragma unroll
            for (int m = 0; m < 4; ++m) {
                int a = 16 * n + r;
                int ib = 60 - 16 * m - 4 * kq;
                u32x2 kk = *(const u32x2*)(kvl + (31 - a) * 144 + ib * 2);
                float kvf[4] = {hi2f(kk[1]), lo2f(kk[1]), hi2f(kk[0]), lo2f(kk[0])};
                float yo[4];
#pragma unroll
                for (int j = 0; j < 4; ++j) yo[j] = rbf(acc[n][m][j]) + rbf(kvf[j] * Dv);
                u32x2 o = {pack2(yo[0], yo[1]), pack2(yo[2], yo[3])};
                *(u32x2*)(ybuf + w * YCH + a * 136 + (16 * m + 4 * kq) * 2) = o;
            }
        __syncthreads();
#pragma unroll
        for (int it = 0; it < 8; ++it) {
            int s = tid + 256 * it;
            const char* yp = ybuf + (s >> 6) * 136 + (s & 63) * 2;
            u32x2 yo;
            yo[0] = (u32)*(const u16*)(yp) | ((u32)*(const u16*)(yp + YCH) << 16);
            yo[1] = (u32)*(const u16*)(yp + 2 * YCH) | ((u32)*(const u16*)(yp + 3 * YCH) << 16);
            float mb = qst[it][0], rinv = qst[it][1];
            u32x2 xo;
#pragma unroll
            for (int j = 0; j < 2; ++j) {
                float a = lo2f(qv[it][j]), b = hi2f(qv[it][j]);
                float qa = rbf(rbf(a - mb) * rinv), qb = rbf(rbf(b - mb) * rinv);
                float ga = rbf(qa * lo2f(yo[j])), gb = rbf(qb * hi2f(yo[j]));
                xo[j] = pack2(a + ga, b + gb);
            }
            long idx = xf_idx(s, bid * 4);
            *(u32x2*)(yF + idx) = yo;
            *(u32x2*)(xaF + idx) = xo;
        }
    } else if (bid < NCONV + NREP1) {
        const int ks = bid - NCONV;
        u16* T = (u16*)lds;
        constexpr int TS = 1736;
        u32x4 t[27];
#pragma unroll
        for (int it = 0; it < 27; ++it) {
            int i = tid + 256 * it;
            int mat = i / 3456, rem = i % 3456, kr = rem / 216, cc = rem % 216;
            t[it] = *(const u32x4*)((mat ? wu : wg) + (long)(16 * ks + kr) * F_ + 8 * cc);
        }
#pragma unroll
        for (int it = 0; it < 27; ++it) {
            int i = tid + 256 * it;
            int mat = i / 3456, rem = i % 3456, kr = rem / 216, cc = rem % 216;
            *(u32x4*)(T + (mat * 16 + kr) * TS + 8 * cc) = t[it];
        }
        __syncthreads();
#pragma unroll 9
        for (int it = 0; it < 27; ++it) {
            int i = tid + 256 * it;
            int uu = i >> 6, l = i & 63;
            int ii = l & 31, hh = l >> 5;
            int mat = ii >> 4, col = 16 * uu + (ii & 15);
            const u16* tp = T + (mat * 16 + 8 * hh) * TS + col;
            u32x4 o;
#pragma unroll
            for (int j = 0; j < 4; ++j) o[j] = (u32)tp[(2 * j) * TS] | ((u32)tp[(2 * j + 1) * TS] << 16);
            *(u32x4*)(W1p + ((long)(uu * KS1 + ks) * 64 + l) * 8) = o;
        }
    } else if (bid < NCONV + NREP1 + NREP2) {
        const int s0 = 2 * (bid - NCONV - NREP1);
        u16* T = (u16*)lds;
        constexpr int TS = 648;
        u32x4 t[20];
#pragma unroll
        for (int it = 0; it < 20; ++it) {
            int i = tid + 256 * it;
            int fr = i / 80, cc = i % 80;
            t[it] = *(const u32x4*)(wd + (long)(32 * s0 + fr) * D_ + 8 * cc);
        }
#pragma unroll
        for (int it = 0; it < 20; ++it) {
            int i = tid + 256 * it;
            int fr = i / 80, cc = i % 80;
            *(u32x4*)(T + fr * TS + 8 * cc) = t[it];
        }
        __syncthreads();
#pragma unroll 10
        for (int it = 0; it < 20; ++it) {
            int i = tid + 256 * it;
            int sl = i / 2560, rem = i % 2560;
            int nt = rem >> 6, l = rem & 63;
            int idx = l & 15, kq = l >> 4;
            int fb = 32 * sl + 16 * (kq >> 1) + 4 * (kq & 1);
            const u16* tp = T + 16 * nt + idx;
            u32x4 o;
            o[0] = (u32)tp[(fb + 0) * TS] | ((u32)tp[(fb + 1) * TS] << 16);
            o[1] = (u32)tp[(fb + 2) * TS] | ((u32)tp[(fb + 3) * TS] << 16);
            o[2] = (u32)tp[(fb + 8) * TS] | ((u32)tp[(fb + 9) * TS] << 16);
            o[3] = (u32)tp[(fb + 10) * TS] | ((u32)tp[(fb + 11) * TS] << 16);
            *(u32x4*)(W2p + ((long)(nt * KS2 + s0 + sl) * 64 + l) * 8) = o;
        }
    } else {
        const int cb = bid - NCONV - NREP1 - NREP2;
        const int cp = tid & 31, kg = tid >> 5;
        float a0 = 0.f, a1 = 0.f, b0 = 0.f, b1 = 0.f;
#pragma unroll 20
        for (int i = 0; i < 80; ++i) {
            u32 wv = *(const u32*)(wg + (long)(kg * 80 + i) * F_ + cb * 64 + 2 * cp);
            u32 uv = *(const u32*)(wu + (long)(kg * 80 + i) * F_ + cb * 64 + 2 * cp);
            a0 += lo2f(wv); a1 += hi2f(wv);
            b0 += lo2f(uv); b1 += hi2f(uv);
        }
        float* T = (float*)lds;
        T[kg * 64 + 2 * cp] = a0;
        T[kg * 64 + 2 * cp + 1] = a1;
        T[512 + kg * 64 + 2 * cp] = b0;
        T[512 + kg * 64 + 2 * cp + 1] = b1;
        __syncthreads();
        if (tid < 128) {
            int mat = tid >> 6, c = tid & 63;
            float t = 0.f;
#pragma unroll
            for (int g = 0; g < 8; ++g) t += T[mat * 512 + g * 64 + c];
            int f = cb * 64 + c;
            cs[(f >> 4) * 32 + mat * 16 + (f & 15)] = t;
        }
    }
}

constexpr int G1_STAGE = 44 * 1024;
constexpr int G1_CS_OFF = 3 * G1_STAGE;

typedef __attribute__((address_space(1))) unsigned long long gu64;
typedef __attribute__((address_space(1))) unsigned int gu32;
typedef __amdgpu_buffer_rsrc_t rsrc_t;
__device__ __forceinline__ rsrc_t mk_rsrc(const void* p, u32 bytes) {
    return __builtin_amdgcn_make_buffer_rsrc((void*)p, 0, bytes, 0x00020000);
}
__device__ __forceinline__ void st16_dev(rsrc_t r, u32 off, u32x4 v) { __builtin_amdgcn_raw_buffer_store_b128(v, r, off, 0, 16); }
__device__ __forceinline__ u32x4 ld16_dev(rsrc_t r, u32 off) { return __builtin_amdgcn_raw_buffer_load_b128(r, off, 0, 16); }

__device__ __forceinline__ void wait_count(u32* cnt, u32 target) {
    for (unsigned spins = 0; spins < (1u << 22); ++spins) {
        u32 v = __hip_atomic_load((gu32*)cnt, __ATOMIC_RELAXED, __HIP_MEMORY_SCOPE_AGENT);
        if (v >= target) break;
        __builtin_amdgcn_s_sleep(2);
    }
    __builtin_amdgcn_fence(__ATOMIC_ACQUIRE, "wavefront");
    asm volatile("" ::: "memory");
}
__device__ __forceinline__ void signal_count(u32* cnt) {
    asm volatile("s_waitcnt vmcnt(0)" ::: "memory");
    __syncthreads();
    if (threadIdx.x == 0) __hip_atomic_fetch_add((gu32*)cnt, 1u, __ATOMIC_RELAXED, __HIP_MEMORY_SCOPE_AGENT);
}

__device__ __forceinline__ void g1_epi_unit(const f32x16& a, const char* cp, float mb, float rinv, rsrc_t hr,
                                            int token, int u, int hh) {
    f32x4 c0 = *(const f32x4*)(cp), c1 = *(const f32x4*)(cp + 32);
    f32x4 c2 = *(const f32x4*)(cp + 64), c3 = *(const f32x4*)(cp + 96);
    float hv[8];
#pragma unroll
    for (int e = 0; e < 8; ++e) {
        float cg = (e < 4) ? c0[e & 3] : c1[e & 3];
        float cu = (e < 4) ? c2[e & 3] : c3[e & 3];
        u32 gu = pack2((a[e] - mb * cg) * rinv, (a[e + 8] - mb * cu) * rinv);
        float g = lo2f(gu), uu = hi2f(gu);
        float sg = rbf(__builtin_amdgcn_rcpf(1.0f + __expf(-g)));
        hv[e] = sg * uu;
    }
    u32x4 o = {pack2(hv[0], hv[1]), pack2(hv[2], hv[3]), pack2(hv[4], hv[5]), pack2(hv[6], hv[7])};
    u32 idx = (u32)(((token >> 4) * KS2 + (u >> 1)) * 64 + (token & 15) + 16 * (2 * (u & 1) + hh));
    st16_dev(hr, idx * 16, o);
}

constexpr int G1_ST_OFF = G1_CS_OFF + 1024;
constexpr int FFN_LDS = 3 * G1_STAGE + 1024 + 4096;
constexpr u32 HP_BYTES = 128u * KS2 * 1024u;
constexpr u32 X_BYTES = (u32)S_ * D_ * 2u;
__device__ __forceinline__ void g1_stats(const char* lds, int tb, int tk, float& mb, float& rinv) {
    f32x2 a = *(const f32x2*)(lds + G1_ST_OFF + (tb * 64 + tk) * 8);
    f32x2 b = *(const f32x2*)(lds + G1_ST_OFF + (tb * 64 + tk + 32) * 8);
    f32x2 c = *(const f32x2*)(lds + G1_ST_OFF + 2048 + (tb * 64 + tk) * 8);
    f32x2 d = *(const f32x2*)(lds + G1_ST_OFF + 2048 + (tb * 64 + tk + 32) * 8);
    float s1 = (a[0] + b[0]) + (c[0] + d[0]), s2 = (a[1] + b[1]) + (c[1] + d[1]);
    const float mean = s1 * (1.0f / 640.0f);
    float var = s2 * (1.0f / 640.0f) - mean * mean;
    var = var > 0.f ? var : 0.f;
    ln_finish(mean, var, mb, rinv);
}

template <bool WAIT>
__device__ __forceinline__ void g1_phase(char* lds, const u16* __restrict__ xF, const u16* __restrict__ W1p,
                                         const float* __restrict__ cs, u16* __restrict__ hp, u32* wcnt) {
    const int tid = threadIdx.x, lane = tid & 63;
    const int w = __builtin_amdgcn_readfirstlane(tid >> 6);
    const int xcd = blockIdx.x & 7, jj = blockIdx.x >> 3;
    const int rt = 8 * (xcd >> 2) + (jj & 7), ct = 4 * (xcd & 3) + (jj >> 3);
    const int ubase = ct * 7;
    const int tb = w & 3;
    const int tk = lane & 31, hh = lane >> 5;
    const int token = 128 * rt + 32 * tb + tk;
    const rsrc_t hr = mk_rsrc(hp, HP_BYTES);
    u32 lb[3];
#pragma unroll
    for (int b = 0; b < 3; ++b) { lb[b] = (u32)(b * G1_STAGE + lane * 16); asm volatile("" : "+v"(lb[b])); }

    if (w >= 4) {
        const int lw = w - 4;
        const char* wbase = (const char*)W1p;
        const char* xbase = (const char*)xF;
        u32 srcw[7];
        const u32 srcx = (u32)((((4 * rt + lw) * KS1) * 64 + lane) * 16);
#pragma unroll
        for (int i = 0; i < 7; ++i) {
            int q = lw * 7 + i;
            int uu = ubase + (q >> 2);
            if (uu > NUNIT - 1) uu = NUNIT - 1;
            srcw[i] = (u32)(((uu * KS1 + (q & 3)) * 64 + lane) * 16);
        }
#define G1_ISSUE_W(s) do { \
        _Pragma("unroll") for (int i = 0; i < 7; ++i) \
            __builtin_amdgcn_global_load_lds((const u32*)(wbase + (srcw[i] + (u32)((s) * 4096))), \
                (u32*)(lds + ((s) % 3) * G1_STAGE + (16 + lw * 7 + i) * 1024 + lane * 16), 16, 0, 0); \
        } while (0)
#define G1_ISSUE_X(s) do { \
        _Pragma("unroll") for (int i = 0; i < 4; ++i) \
            __builtin_amdgcn_global_load_lds((const u32*)(xbase + (srcx + (u32)((s) * 4096 + i * 1024))), \
                (u32*)(lds + ((s) % 3) * G1_STAGE + (lw * 4 + i) * 1024 + lane * 16), 16, 0, WAIT ? 16 : 0); \
        } while (0)
        G1_ISSUE_W(0);
        G1_ISSUE_W(1);
        if (WAIT) wait_count(wcnt, 16);
        G1_ISSUE_X(0);
        G1_ISSUE_X(1);
        float sa[4] = {0.f, 0.f, 0.f, 0.f}, sb[4] = {0.f, 0.f, 0.f, 0.f};
#pragma unroll
        for (int s = 0; s < 10; ++s) {
            if (s == 0) asm volatile("s_waitcnt vmcnt(4) lgkmcnt(0)" ::: "memory");
            else if (s < 9) asm volatile("s_waitcnt vmcnt(11) lgkmcnt(0)" ::: "memory");
            else asm volatile("s_waitcnt vmcnt(0) lgkmcnt(0)" ::: "memory");
            __builtin_amdgcn_s_barrier();
            asm volatile("" ::: "memory");
            u32x4 xx[2];
#pragma unroll
            for (int j = 0; j < 2; ++j) xx[j] = *(const u32x4*)(lds + lb[s % 3] + tb * 4096 + j * 1024);
            if (s + 2 < 10) { G1_ISSUE_W(s + 2); G1_ISSUE_X(s + 2); }
#pragma unroll
            for (int j = 0; j < 2; ++j)
#pragma unroll
                for (int e = 0; e < 4; ++e) {
                    float lo = lo2f(xx[j][e]), hi = hi2f(xx[j][e]);
                    sa[e] += lo + hi;
                    sb[e] = __builtin_fmaf(lo, lo, __builtin_fmaf(hi, hi, sb[e]));
                }
        }
#undef G1_ISSUE_W
#undef G1_ISSUE_X
        float s1 = (sa[0] + sa[1]) + (sa[2] + sa[3]), s2 = (sb[0] + sb[1]) + (sb[2] + sb[3]);
        { f32x2 st = {s1, s2}; *(f32x2*)(lds + G1_ST_OFF + (tb * 64 + lane) * 8) = st; }
        __syncthreads();
        float mb, rinv;
        g1_stats(lds, tb, tk, mb, rinv);
#pragma unroll
        for (int i = 4; i < 7; ++i) {
            int u = ubase + i;
            if (u < NUNIT) {
                f32x16 a;
#pragma unroll
                for (int r = 0; r < 4; ++r) {
                    f32x4 t = *(const f32x4*)(lds + G1_STAGE + (tb * 12 + (i - 4) * 4 + r) * 1024 + lane * 16);
                    a[4 * r] = t[0]; a[4 * r + 1] = t[1]; a[4 * r + 2] = t[2]; a[4 * r + 3] = t[3];
                }
                g1_epi_unit(a, lds + G1_CS_OFF + i * 128 + hh * 16, mb, rinv, hr, token, u, hh);
            }
        }
    } else {
        if (lane < 56) {
            int e = ubase * 32 + w * 56 + lane;
            float csv = (e < NUNIT * 32) ? cs[e] : 0.f;
            *(float*)(lds + G1_CS_OFF + (w * 56 + lane) * 4) = csv;
        }
        f32x16 acc[7];
#pragma unroll
        for (int a = 0; a < 7; ++a)
#pragma unroll
            for (int i = 0; i < 16; ++i) acc[a][i] = 0.f;
        bf16x8 xf[3], wf[3][7];
        float sa[2] = {0.f, 0.f}, sb[2] = {0.f, 0.f};
#pragma unroll
        for (int g = 0; g < 42; ++g) {
            const int s = g >> 2, j = g & 3;
            if (g < 40) {
                if (j == 0) {
                    asm volatile("s_waitcnt lgkmcnt(0)" ::: "memory");
                    __builtin_amdgcn_s_barrier();
                    asm volatile("" ::: "memory");
                }
                xf[g % 3] = *(const bf16x8*)(lds + lb[s % 3] + tb * 4096 + j * 1024);
#pragma unroll
                for (int i = 0; i < 7; ++i) wf[g % 3][i] = *(const bf16x8*)(lds + lb[s % 3] + (4 + i) * 4096 + j * 1024);
            }
            if (g >= 2) {
                const int c = (g - 2) % 3;
#pragma unroll
                for (int i = 0; i < 7; ++i)
                    acc[i] = __builtin_amdgcn_mfma_f32_32x32x16_bf16(wf[c][i], xf[c], acc[i], 0, 0, 0);
                if (((g - 2) & 3) >= 2) {
                    u32x4 xx = __builtin_bit_cast(u32x4, xf[c]);
#pragma unroll
                    for (int e = 0; e < 4; ++e) {
                        float lo = lo2f(xx[e]), hi = hi2f(xx[e]);
                        sa[e & 1] += lo + hi;
                        sb[e & 1] = __builtin_fmaf(lo, lo, __builtin_fmaf(hi, hi, sb[e & 1]));
                    }
                }
#pragma unroll
                for (int i = 0; i < 7; ++i) {
                    __builtin_amdgcn_sched_group_barrier(0x8, 1, 0);
                    __builtin_amdgcn_sched_group_barrier(0x100, 1, 0);
                    __builtin_amdgcn_sched_group_barrier(0x2, 4, 0);
                }
            }
            __builtin_amdgcn_sched_barrier(0);
        }
#pragma unroll
        for (int i = 4; i < 7; ++i)
#pragma unroll
            for (int r = 0; r < 4; ++r) {
                f32x4 t = {acc[i][4 * r], acc[i][4 * r + 1], acc[i][4 * r + 2], acc[i][4 * r + 3]};
                *(f32x4*)(lds + G1_STAGE + (tb * 12 + (i - 4) * 4 + r) * 1024 + lane * 16) = t;
            }
        { f32x2 st = {sa[0] + sa[1], sb[0] + sb[1]}; *(f32x2*)(lds + G1_ST_OFF + 2048 + (tb * 64 + lane) * 8) = st; }
        __syncthreads();
        float mb, rinv;
        g1_stats(lds, tb, tk, mb, rinv);
#pragma unroll
        for (int i = 0; i < 4; ++i) {
            int u = ubase + i;
            if (u < NUNIT) g1_epi_unit(acc[i], lds + G1_CS_OFF + i * 128 + hh * 16, mb, rinv, hr, token, u, hh);
        }
    }
}

constexpr int PROW = 336;
constexpr int PTILE = 64 * PROW;
constexpr int PD2 = 3;

template <bool MID>
__device__ __forceinline__ void g2_phase(char* lds, const u16* __restrict__ hp, const u16* __restrict__ W2p,
                                         const u16* __restrict__ xresF, const u16* __restrict__ yF,
                                         u16* __restrict__ xout, unsigned long long* gran, u32* wcnt) {
    const int tid = threadIdx.x, lane = tid & 63;
    const int w = __builtin_amdgcn_readfirstlane(tid >> 6);
    const int xcd = blockIdx.x & 7, jj = blockIdx.x >> 3;
    const int rt = 8 * (xcd >> 1) + (jj & 7), ct = 4 * (xcd & 1) + (jj >> 3);

    const rsrc_t hr = mk_rsrc(hp, HP_BYTES);
    const u32 hoff = (u32)(((4 * rt) * KS2 * 64 + lane) * 16);
    const bf16x8* wbp = (const bf16x8*)W2p + (long)(5 * ct) * KS2 * 64 + lane;
    const int nsteps = (w < 6) ? 7 : 6;
#define LDH(m, s) __builtin_bit_cast(bf16x8, ld16_dev(hr, hoff + (u32)(((m) * KS2 + (s)) * 1024)))

    bf16x8 hf[PD2][4], wf[PD2][5];
    char* wl = lds + w * 15360 + lane * 16;
#pragma unroll
    for (int i = 0; i < PD2; ++i) {
        int s = w + 8 * i;
#pragma unroll
        for (int n = 0; n < 5; ++n) wf[i][n] = wbp[(long)(n * KS2 + s) * 64];
    }
#pragma unroll
    for (int i = 3; i < 6; ++i) {
        int s = w + 8 * i;
#pragma unroll
        for (int n = 0; n < 5; ++n)
            __builtin_amdgcn_global_load_lds((const u32*)(wbp + (long)(n * KS2 + s) * 64),
                                             (u32*)(wl + ((i - 3) * 5 + n) * 1024), 16, 0, 0);
    }
    const int tok = lane;
    const int token = 64 * rt + tok;
    const rsrc_t xr = mk_rsrc(xresF, X_BYTES);
    u32x4 res[2], yv[2];
#pragma unroll
    for (int it = 0; it < 2; ++it) {
        int cc = w + 8 * it;
        if (cc < 10) {
            long idx = xf_idx(token, 80 * ct + 8 * cc);
            res[it] = ld16_dev(xr, (u32)idx * 2);
            if (MID) yv[it] = *(const u32x4*)(yF + idx);
        }
    }
    wait_count(wcnt, 16);
#pragma unroll
    for (int i = 0; i < PD2; ++i) {
        int s = w + 8 * i;
#pragma unroll
        for (int m = 0; m < 4; ++m) hf[i][m] = LDH(m, s);
    }
    f32x4 acc[5][4];
#pragma unroll
    for (int n = 0; n < 5; ++n)
#pragma unroll
        for (int m = 0; m < 4; ++m) acc[n][m] = (f32x4){0.f, 0.f, 0.f, 0.f};
#pragma unroll
    for (int i = 0; i < 7; ++i) {
        if (i < nsteps) {
#pragma unroll
            for (int n = 0; n < 5; ++n)
#pragma unroll
                for (int m = 0; m < 4; ++m)
                    acc[n][m] = __builtin_amdgcn_mfma_f32_16x16x32_bf16(wf[i % PD2][n], hf[i % PD2][m], acc[n][m], 0, 0, 0);
            if (i + PD2 < nsteps) {
                int s = w + 8 * (i + PD2);
#pragma unroll
                for (int m = 0; m < 4; ++m) hf[i % PD2][m] = LDH(m, s);
                if (i + PD2 < 6) {
#pragma unroll
                    for (int n = 0; n < 5; ++n) wf[i % PD2][n] = *(const bf16x8*)(wl + (i * 5 + n) * 1024);
                } else {
#pragma unroll
                    for (int n = 0; n < 5; ++n) wf[i % PD2][n] = wbp[(long)(n * KS2 + s) * 64];
                }
            }
        }
    }
    __syncthreads();
#undef LDH
    const int tl = lane & 15, q = lane >> 4;
    char* slot = lds + (w & 3) * PTILE + tl * PROW + q * 16;
    if (w >= 4) {
#pragma unroll
        for (int n = 0; n < 5; ++n)
#pragma unroll
            for (int m = 0; m < 4; ++m) *(f32x4*)(slot + m * 16 * PROW + n * 64) = acc[n][m];
    }
    __syncthreads();
    if (w < 4) {
#pragma unroll
        for (int n = 0; n < 5; ++n)
#pragma unroll
            for (int m = 0; m < 4; ++m) {
                f32x4 o = *(const f32x4*)(slot + m * 16 * PROW + n * 64);
                *(f32x4*)(slot + m * 16 * PROW + n * 64) = o + acc[n][m];
            }
    }
    __syncthreads();
    u32x4 x2[2];
#pragma unroll
    for (int it = 0; it < 2; ++it) {
        int cc = w + 8 * it;
        if (cc < 10) {
            const char* p = lds + tok * PROW + cc * 32;
            f32x4 s0 = *(const f32x4*)(p), s1 = *(const f32x4*)(p + 16);
#pragma unroll
            for (int k = 1; k < 4; ++k) {
                s0 += *(const f32x4*)(p + k * PTILE);
                s1 += *(const f32x4*)(p + k * PTILE + 16);
            }
            u32x4 rs = res[it];
            x2[it][0] = pack2(lo2f(rs[0]) + rbf(s0[0]), hi2f(rs[0]) + rbf(s0[1]));
            x2[it][1] = pack2(lo2f(rs[1]) + rbf(s0[2]), hi2f(rs[1]) + rbf(s0[3]));
            x2[it][2] = pack2(lo2f(rs[2]) + rbf(s1[0]), hi2f(rs[2]) + rbf(s1[1]));
            x2[it][3] = pack2(lo2f(rs[3]) + rbf(s1[2]), hi2f(rs[3]) + rbf(s1[3]));
            if (!MID) *(u32x4*)(xout + (long)token * D_ + 80 * ct + 8 * cc) = x2[it];
        }
    }
    if (MID) {
        __syncthreads();
        float* red = (float*)lds;
        float* red2 = (float*)(lds + 8192);
#pragma unroll
        for (int it = 0; it < 2; ++it) {
            int cc = w + 8 * it;
            if (cc < 10) {
                float a = 0.f, b = 0.f;
#pragma unroll
                for (int e = 0; e < 4; ++e) {
                    float lo = lo2f(x2[it][e]), hi = hi2f(x2[it][e]);
                    a += lo + hi;
                    b = __builtin_fmaf(lo, lo, __builtin_fmaf(hi, hi, b));
                }
                red[cc * 64 + tok] = a;
                red[640 + cc * 64 + tok] = b;
            }
        }
        __syncthreads();
        if (w < 2) {
            float t = 0.f;
#pragma unroll
            for (int c = 0; c < 10; ++c) t += red[w * 640 + c * 64 + tok];
            unsigned long long g = ((unsigned long long)GTAG << 32) | (unsigned long long)__float_as_uint(t);
            __hip_atomic_store((gu64*)(gran + ((rt * 8 + ct) * 2 + w) * 64 + tok), g, __ATOMIC_RELAXED, __HIP_MEMORY_SCOPE_AGENT);
        }
        {
            unsigned long long g0, g1;
            gu64* p0 = (gu64*)(gran + ((rt * 8 + w) * 2 + 0) * 64 + tok);
            gu64* p1 = (gu64*)(gran + ((rt * 8 + w) * 2 + 1) * 64 + tok);
            for (unsigned spins = 0; spins < (1u << 22); ++spins) {
                g0 = __hip_atomic_load(p0, __ATOMIC_RELAXED, __HIP_MEMORY_SCOPE_AGENT);
                g1 = __hip_atomic_load(p1, __ATOMIC_RELAXED, __HIP_MEMORY_SCOPE_AGENT);
                bool ok = ((u32)(g0 >> 32) == GTAG) && ((u32)(g1 >> 32) == GTAG);
                if (__all(ok)) break;
                __builtin_amdgcn_s_sleep(1);
            }
            red2[w * 64 + tok] = __uint_as_float((u32)g0);
            red2[512 + w * 64 + tok] = __uint_as_float((u32)g1);
        }
        __syncthreads();
        float a = 0.f, b = 0.f;
#pragma unroll
        for (int c = 0; c < 8; ++c) { a += red2[c * 64 + tok]; b += red2[512 + c * 64 + tok]; }
        const float mean = a * (1.0f / 640.0f);
        float var = b * (1.0f / 640.0f) - mean * mean;
        var = var > 0.f ? var : 0.f;
        float mb, rinv;
        ln_finish(mean, var, mb, rinv);
        const rsrc_t xo = mk_rsrc(xout, X_BYTES);
#pragma unroll
        for (int it = 0; it < 2; ++it) {
            int cc = w + 8 * it;
            if (cc < 10) {
                u32x4 o;
#pragma unroll
                for (int e = 0; e < 4; ++e) {
                    float xa = lo2f(x2[it][e]), xb = hi2f(x2[it][e]);
                    float qa = rbf(rbf(xa - mb) * rinv), qb = rbf(rbf(xb - mb) * rinv);
                    float ga = rbf(qa * lo2f(yv[it][e])), gb = rbf(qb * hi2f(yv[it][e]));
                    o[e] = pack2(xa + ga, xb + gb);
                }
                st16_dev(xo, (u32)xf_idx(token, 80 * ct + 8 * cc) * 2, o);
            }
        }
    }
}

__global__ __launch_bounds__(512, 2) void ffn_kernel(const u16* __restrict__ xaF, const u16* __restrict__ W1p,
                                                     const float* __restrict__ cs, u16* __restrict__ hpA,
                                                     u16* __restrict__ hpB, const u16* __restrict__ W2p,
                                                     const u16* __restrict__ yF, u16* __restrict__ xbF,
                                                     u16* __restrict__ out, unsigned long long* gran, u32* cnt) {
    __shared__ __attribute__((aligned(16))) char lds[FFN_LDS];
    const int xcd = blockIdx.x & 7, jj = blockIdx.x >> 3;
    const int rt1 = 8 * (xcd >> 2) + (jj & 7);
    const int rg2 = (8 * (xcd >> 1) + (jj & 7)) >> 1;
    g1_phase<false>(lds, xaF, W1p, cs, hpA, nullptr);
    signal_count(cnt + (0 * 16 + rt1) * 32);
    g2_phase<true>(lds, hpA, W2p, xaF, yF, xbF, gran, cnt + (0 * 16 + rg2) * 32);
    signal_count(cnt + (1 * 16 + rg2) * 32);
    g1_phase<true>(lds, xbF, W1p, cs, hpB, cnt + (1 * 16 + rt1) * 32);
    signal_count(cnt + (2 * 16 + rt1) * 32);
    g2_phase<false>(lds, hpB, W2p, xbF, yF, out, gran, cnt + (2 * 16 + rg2) * 32);
}

extern "C" void kernel_launch(void* const* d_in, const int* in_sizes, int n_in,
                              void* d_out, int out_size, void* d_ws, size_t ws_size,
                              hipStream_t stream) {
    const u16* q = (const u16*)d_in[0];
    const u16* k = (const u16*)d_in[1];
    const u16* v = (const u16*)d_in[2];
    const u16* sk = (const u16*)d_in[3];
    const u16* ssmk = (const u16*)d_in[4];
    const u16* ssmD = (const u16*)d_in[5];
    const u16* wg = (const u16*)d_in[7];
    const u16* wu = (const u16*)d_in[8];
    const u16* wd = (const u16*)d_in[9];
    u16* out = (u16*)d_out;

    char* ws = (char*)d_ws;
    constexpr size_t SZ_X = (size_t)S_ * D_ * 2;
    size_t off = 0;
    unsigned long long* gran = (unsigned long long*)(ws + off); off += (size_t)GRAN_WORDS * 8;
    u32* cnt = (u32*)(ws + off); off += 8192;
    u16* W1p = (u16*)(ws + off); off += (size_t)NUNIT * KS1 * 1024;
    u16* kvR = (u16*)(ws + off); off += SZ_X;
    u16* yF = (u16*)(ws + off); off += SZ_X;
    u16* xaF = (u16*)(ws + off); off += SZ_X;
    u16* xbF = (u16*)(ws + off); off += SZ_X;
    u16* qF = (u16*)(ws + off); off += SZ_X;
    u16* W2p = (u16*)(ws + off); off += (size_t)40 * KS2 * 1024;
    u16* hpA = (u16*)(ws + off); off += (size_t)128 * KS2 * 1024;
    u16* hpB = (u16*)(ws + off); off += (size_t)128 * KS2 * 1024;
    f32x2* qs = (f32x2*)(ws + off); off += (size_t)S_ * 8;
    float* cs = (float*)(ws + off); off += (size_t)NUNIT * 32 * 4;

    prep_kernel<<<320 + 512, 256, 0, stream>>>(k, v, sk, kvR, q, qF, qs, (u32x4*)gran);
    conv_kernel<<<NCONV + NREP1 + NREP2 + NCS, 256, 0, stream>>>(kvR, ssmk, ssmD, yF, qF, qs, xaF, wg, wu, wd, W1p, W2p, cs);
    ffn_kernel<<<256, 512, 0, stream>>>(xaF, W1p, cs, hpA, hpB, W2p, yF, xbF, out, gran, cnt);
}
```

```cpp
#include <hip/hip_runtime.h>
#include <hip/hip_bf16.h>
#include <stdint.h>

typedef unsigned short u16;
typedef unsigned int u32;
typedef __attribute__((ext_vector_type(8))) short bf16x8;
typedef __attribute__((ext_vector_type(4))) float f32x4;
typedef __attribute__((ext_vector_type(16))) float f32x16;
typedef __attribute__((ext_vector_type(4))) u32 u32x4;
typedef __attribute__((ext_vector_type(2))) u32 u32x2;
typedef __attribute__((ext_vector_type(2))) float f32x2;
typedef __attribute__((ext_vector_type(2))) __bf16 bf16x2v;

constexpr int S_ = 2048, D_ = 640, F_ = 1728;
constexpr int NUNIT = 108;
constexpr int KS1 = 40;
constexpr int KS2 = 54;

__device__ __forceinline__ float lo2f(u32 v) { return __uint_as_float(v << 16); }
__device__ __forceinline__ float hi2f(u32 v) { return __uint_as_float(v & 0xffff0000u); }
__device__ __forceinline__ u32 pack2(float a, float b) {
    f32x2 f = {a, b};
    bf16x2v r = __builtin_convertvector(f, bf16x2v);
    return __builtin_bit_cast(u32, r);
}
__device__ __forceinline__ float rbf(float a) {
    __bf16 b = (__bf16)a;
    return __uint_as_float(((u32)__builtin_bit_cast(u16, b)) << 16);
}
__device__ __forceinline__ u16 f2bf(float a) {
    __bf16 b = (__bf16)a;
    return __builtin_bit_cast(u16, b);
}

__device__ __forceinline__ float rowsum16(float x) {
    x += __builtin_bit_cast(float, __builtin_amdgcn_update_dpp(0, __builtin_bit_cast(int, x), 0x128, 0xf, 0xf, false));
    x += __builtin_bit_cast(float, __builtin_amdgcn_update_dpp(0, __builtin_bit_cast(int, x), 0x124, 0xf, 0xf, false));
    x += __builtin_bit_cast(float, __builtin_amdgcn_update_dpp(0, __builtin_bit_cast(int, x), 0x122, 0xf, 0xf, false));
    x += __builtin_bit_cast(float, __builtin_amdgcn_update_dpp(0, __builtin_bit_cast(int, x), 0x121, 0xf, 0xf, false));
    return x;
}

__device__ __forceinline__ float wavesum(float x) {
    x = rowsum16(x);
    x += __shfl_xor(x, 16);
    x += __shfl_xor(x, 32);
    return x;
}
__device__ __forceinline__ long xf_idx(int token, int d) {
    return ((long)((token >> 5) * 80 + (d >> 3)) * 32 + (token & 31)) * 8 + (d & 7);
}
__device__ __forceinline__ float dot2bf(u32 a, u32 b, float c) {
    return __builtin_amdgcn_fdot2_f32_bf16(__builtin_bit_cast(bf16x2v, a), __builtin_bit_cast(bf16x2v, b), c, false);
}
__device__ __forceinline__ void ln_finish(float mean, float var, float& mb, float& rinv) {
    mb = rbf(mean);
    float den = rbf(__builtin_sqrtf(rbf(rbf(var) + 1e-5f)));
    rinv = 1.0f / den;
}
constexpr u32 GTAG = 0x13572468u;
constexpr int GRAN_WORDS = 32 * 8 * 2 * 64;

constexpr int NREP1 = 40;
constexpr int NREP2 = 27;
constexpr int NCS = 27;
__global__ __launch_bounds__(256) void prep_kernel(const u16* __restrict__ k, const u16* __restrict__ v,
                                                   const u16* __restrict__ sk, u16* __restrict__ kvR,
                                                   const u16* __restrict__ q, u16* __restrict__ qF,
                                                   f32x2* __restrict__ qs, u32x4* __restrict__ gran) {
    __shared__ __attribute__((aligned(16))) u32 T[64 * 32];
    if (blockIdx.x >= 320) {
        const int b = blockIdx.x - 320;
        if (b < 66) { u32x4 z = {0u, 0u, 0u, 0u}; gran[b * 256 + threadIdx.x] = z; }
        const int lane = threadIdx.x & 63;
        const int row = b * 4 + (threadIdx.x >> 6);
        const u32* xs = (const u32*)(q + (long)row * D_);
        u32 xv[5];
#pragma unroll
        for (int m = 0; m < 5; ++m) xv[m] = xs[lane + 64 * m];
        float sum = 0.f;
#pragma unroll
        for (int m = 0; m < 5; ++m) sum += lo2f(xv[m]) + hi2f(xv[m]);
        sum = wavesum(sum);
        float mean = sum * (1.0f / 640.0f);
        float sq = 0.f;
#pragma unroll
        for (int m = 0; m < 5; ++m) {
            float a = lo2f(xv[m]) - mean, c = hi2f(xv[m]) - mean;
            sq += a * a + c * c;
        }
        sq = wavesum(sq);
        float mb, rinv;
        ln_finish(mean, sq * (1.0f / 640.0f), mb, rinv);
        if (lane == 0) { f32x2 o = {mb, rinv}; qs[row] = o; }
#pragma unroll
        for (int m = 0; m < 5; ++m) *(u32*)(qF + xf_idx(row, 2 * (lane + 64 * m))) = xv[m];
        return;
    }
    const int st = blockIdx.x / 10, dt = blockIdx.x % 10;
    const int s0 = st * 64, d0 = dt * 64;
    const int t = threadIdx.x, dc = t & 7, rp = t >> 3;
    const int s = s0 + 2 * rp, d = d0 + 8 * dc;
    u32x4 kr[5];
#pragma unroll
    for (int i = 0; i < 5; ++i) {
        int row = s - 3 + i;
        u32x4 z = {0u, 0u, 0u, 0u};
        kr[i] = (row >= 0) ? *(const u32x4*)(k + (long)row * D_ + d) : z;
    }
    u32x4 v0 = *(const u32x4*)(v + (long)s * D_ + d);
    u32x4 v1 = *(const u32x4*)(v + (long)(s + 1) * D_ + d);
    u32x4 skv[4];
#pragma unroll
    for (int i = 0; i < 4; ++i) skv[i] = *(const u32x4*)(sk + (long)d * 4 + 8 * i);
    const int p = 31 - rp;
#pragma unroll
    for (int e = 0; e < 8; ++e) {
        u32 w01 = skv[e >> 1][(e & 1) * 2 + 0], w23 = skv[e >> 1][(e & 1) * 2 + 1];
        float c0 = lo2f(w01), c1 = hi2f(w01), c2 = lo2f(w23), c3 = hi2f(w23);
        float kk[5];
#pragma unroll
        for (int i = 0; i < 5; ++i) {
            u32 wv = kr[i][e >> 1];
            kk[i] = (e & 1) ? hi2f(wv) : lo2f(wv);
        }
        float va = (e & 1) ? hi2f(v0[e >> 1]) : lo2f(v0[e >> 1]);
        float vb = (e & 1) ? hi2f(v1[e >> 1]) : lo2f(v1[e >> 1]);
        float a = rbf(kk[0] * c3);
        a = rbf(a + rbf(kk[1] * c2));
        a = rbf(a + rbf(kk[2] * c1));
        a = rbf(a + rbf(kk[3] * c0));
        float b = rbf(kk[1] * c3);
        b = rbf(b + rbf(kk[2] * c2));
        b = rbf(b + rbf(kk[3] * c1));
        b = rbf(b + rbf(kk[4] * c0));
        u32 pk = pack2(b * vb, a * va);
        int dl = 8 * dc + e;
        T[dl * 32 + ((((p >> 2) ^ dc)) << 2) + (p & 3)] = pk;
    }
    __syncthreads();
#pragma unroll
    for (int it = 0; it < 2; ++it) {
        int idx = t + 256 * it;
        int dl = idx >> 3, c = idx & 7;
        u32x4 o = *(const u32x4*)&T[dl * 32 + ((c ^ (dl >> 3)) << 2)];
        *(u32x4*)(kvR + (long)(d0 + dl) * S_ + (1984 - s0) + 8 * c) = o;
    }
}

constexpr int HC_STRIDE = 4416;
constexpr int KV_BYTES = 47 * 144;
constexpr int WAVE_LDS = 4 * HC_STRIDE + KV_BYTES;
constexpr int YBUF_OFF = 4 * WAVE_LDS;
constexpr int YCH = 32 * 136;
constexpr int CONV_LDS = YBUF_OFF + 4 * YCH;
constexpr int NCONV = 160;

__global__ __launch_bounds__(256, 1) void conv_kernel(const u16* __restrict__ kvR, const u16* __restrict__ ssmk,
                                                      const u16* __restrict__ ssmD, u16* __restrict__ yF,
                                                      const u16* __restrict__ qF,
                                                      const f32x2* __restrict__ qs, u16* __restrict__ xaF,
                                                      const u16* __restrict__ wg, const u16* __restrict__ wu,
                                                      const u16* __restrict__ wd, u16* __restrict__ W1p,
                                                      u16* __restrict__ W2p, float* __restrict__ cs) {
    __shared__ __attribute__((aligned(16))) char lds[CONV_LDS];
    const int tid = threadIdx.x, lane = tid & 63, w = tid >> 6;
    const int bid = blockIdx.x;
    if (bid < NCONV) {
        const int ch = bid * 4 + w;
        char* wl = lds + w * WAVE_LDS;
        char* kvl = wl + 4 * HC_STRIDE;
        const u32x4* hsrc = (const u32x4*)(ssmk + (long)ch * S_);
        const u32x4* ksrc = (const u32x4*)(kvR + (long)ch * S_);
        u32x4 kvv[4], ha[5], hb[5];
#pragma unroll
        for (int m = 0; m < 5; ++m) {
            int c = lane - 8 + 64 * m;
            int c0 = c < 0 ? 0 : (c > 255 ? 255 : c);
            int c1 = c + 1 < 0 ? 0 : (c + 1 > 255 ? 255 : c + 1);
            ha[m] = hsrc[c0];
            hb[m] = hsrc[c1];
        }
#pragma unroll
        for (int m = 0; m < 4; ++m) kvv[m] = ksrc[lane + 64 * m];
#pragma unroll
        for (int m = 0; m < 5; ++m) {
            int c = lane - 8 + 64 * m;
            u32x4 z = {0u, 0u, 0u, 0u};
            u32x4 a = (c >= 0) ? ha[m] : z;
            u32x4 b = (c + 1 >= 0 && c + 1 < 256) ? hb[m] : z;
            u32 ww[8] = {a[0], a[1], a[2], a[3], b[0], b[1], b[2], b[3]};
            u32x4 o0, o1, o2, o3;
#pragma unroll
            for (int i = 0; i < 4; ++i) {
                o0[i] = __builtin_amdgcn_alignbit(ww[i + 1], ww[i], 16);
                o1[i] = ww[i + 1];
                o2[i] = __builtin_amdgcn_alignbit(ww[i + 2], ww[i + 1], 16);
                o3[i] = ww[i + 2];
            }
            if (m < 4 || c < 256) {
                char* dst = wl + (c + 8) * 16;
                *(u32x4*)(dst + 0 * HC_STRIDE) = o0;
                *(u32x4*)(dst + 1 * HC_STRIDE) = o1;
                *(u32x4*)(dst + 2 * HC_STRIDE) = o2;
                *(u32x4*)(dst + 3 * HC_STRIDE) = o3;
            }
        }
#pragma unroll
        for (int m = 0; m < 4; ++m) {
            int c = lane + 64 * m;
            *(u32x4*)(kvl + (c >> 3) * 144 + (c & 7) * 16) = kvv[m];
        }
        {
            u32x4 z = {0u, 0u, 0u, 0u};
#pragma unroll
            for (int m = 0; m < 3; ++m) {
                int c = lane + 64 * m;
                if (c < 135) *(u32x4*)(kvl + 32 * 144 + c * 16) = z;
            }
        }
        __builtin_amdgcn_s_waitcnt(0xC07F);
        __builtin_amdgcn_wave_barrier();
        u32x2 qv[8];
        f32x2 qst[8];
#pragma unroll
        for (int it = 0; it < 8; ++it) {
            int s = tid + 256 * it;
            qv[it] = *(const u32x2*)(qF + xf_idx(s, bid * 4));
            qst[it] = qs[s];
        }

        const int r = lane & 15, kq = lane >> 4;
        u32 aoffA = (u32)(w * WAVE_LDS + (r & 3) * HC_STRIDE + 2 * ((r & 12) + 8 * kq));
        u32 aoffB = aoffA + 8, aoffC = aoffA + 32, aoffD = aoffA + 40;
        asm volatile("" : "+v"(aoffB));
        asm volatile("" : "+v"(aoffC));
        asm volatile("" : "+v"(aoffD));
        const u32 boff = (u32)(w * WAVE_LDS + 4 * HC_STRIDE + 16 * (9 * (31 - r) + kq));

        f32x4 acc[2][4];
#pragma unroll
        for (int n = 0; n < 2; ++n)
#pragma unroll
            for (int m = 0; m < 4; ++m) acc[n][m] = (f32x4){0.f, 0.f, 0.f, 0.f};

        bf16x8 A[136], B0[32], B1[64];
#define LOADA(i) do { \
            u32x2 l0 = *(const u32x2*)(lds + (((i) & 1) ? aoffC : aoffA) + 64 * ((i) >> 1)); \
            u32x2 l1 = *(const u32x2*)(lds + (((i) & 1) ? aoffD : aoffB) + 64 * ((i) >> 1)); \
            u32x4 t4 = {l0[0], l0[1], l1[0], l1[1]}; \
            A[i] = __builtin_bit_cast(bf16x8, t4); } while (0)
#define KSO(ks) (16 * (9 * ((ks) >> 1) + 4 * ((ks) & 1)))
#define LOADB(ks) do { \
            B1[ks] = *(const bf16x8*)(lds + boff + KSO(ks) - 16 * 9 * 16); \
            if ((ks) < 32) B0[(ks) < 32 ? (ks) : 0] = *(const bf16x8*)(lds + boff + KSO(ks)); } while (0)
#pragma unroll
        for (int i = 0; i < 6; ++i) LOADA(i);
        LOADB(0);
        LOADB(1);
        __builtin_amdgcn_sched_barrier(0);
#pragma unroll
        for (int ks = 0; ks < 64; ++ks) {
            if (ks + 2 < 64) {
                LOADA(2 * ks + 6);
                LOADA(2 * ks + 7);
                LOADB(ks + 2);
            }
            __builtin_amdgcn_sched_barrier(0);
#pragma unroll
            for (int m = 0; m < 4; ++m)
                acc[1][m] = __builtin_amdgcn_mfma_f32_16x16x32_bf16(A[2 * ks + m], B1[ks], acc[1][m], 0, 0, 0);
            if (ks < 32) {
#pragma unroll
                for (int m = 0; m < 4; ++m)
                    acc[0][m] = __builtin_amdgcn_mfma_f32_16x16x32_bf16(A[2 * ks + m], B0[ks < 32 ? ks : 0], acc[0][m], 0, 0, 0);
            }
            __builtin_amdgcn_sched_barrier(0);
        }
#undef LOADA
#undef LOADB
#undef KSO
        const float Dv = lo2f((u32)ssmD[ch]);
        char* ybuf = lds + YBUF_OFF;
#pragma unroll
        for (int n = 0; n < 2; ++n)
#pragma unroll
            for (int m = 0; m < 4; ++m) {
                int a = 16 * n + r;
                int ib = 60 - 16 * m - 4 * kq;
                u32x2 kk = *(const u32x2*)(kvl + (31 - a) * 144 + ib * 2);
                float kvf[4] = {hi2f(kk[1]), lo2f(kk[1]), hi2f(kk[0]), lo2f(kk[0])};
                float yo[4];
#pragma unroll
                for (int j = 0; j < 4; ++j) yo[j] = rbf(acc[n][m][j]) + rbf(kvf[j] * Dv);
                u32x2 o = {pack2(yo[0], yo[1]), pack2(yo[2], yo[3])};
                *(u32x2*)(ybuf + w * YCH + a * 136 + (16 * m + 4 * kq) * 2) = o;
            }
        __syncthreads();
#pragma unroll
        for (int it = 0; it < 8; ++it) {
            int s = tid + 256 * it;
            const char* yp = ybuf + (s >> 6) * 136 + (s & 63) * 2;
            u32x2 yo;
            yo[0] = (u32)*(const u16*)(yp) | ((u32)*(const u16*)(yp + YCH) << 16);
            yo[1] = (u32)*(const u16*)(yp + 2 * YCH) | ((u32)*(const u16*)(yp + 3 * YCH) << 16);
            float mb = qst[it][0], rinv = qst[it][1];
            u32x2 xo;
#pragma unroll
            for (int j = 0; j < 2; ++j) {
                float a = lo2f(qv[it][j]), b = hi2f(qv[it][j]);
                float qa = rbf(rbf(a - mb) * rinv), qb = rbf(rbf(b - mb) * rinv);
                float ga = rbf(qa * lo2f(yo[j])), gb = rbf(qb * hi2f(yo[j]));
                xo[j] = pack2(a + ga, b + gb);
            }
            long idx = xf_idx(s, bid * 4);
            *(u32x2*)(yF + idx) = yo;
            *(u32x2*)(xaF + idx) = xo;
        }
    } else if (bid < NCONV + NREP1) {
        const int ks = bid - NCONV;
        u16* T = (u16*)lds;
        constexpr int TS = 1736;
        u32x4 t[27];
#pragma unroll
        for (int it = 0; it < 27; ++it) {
            int i = tid + 256 * it;
            int mat = i / 3456, rem = i % 3456, kr = rem / 216, cc = rem % 216;
            t[it] = *(const u32x4*)((mat ? wu : wg) + (long)(16 * ks + kr) * F_ + 8 * cc);
        }
#pragma unroll
        for (int it = 0; it < 27; ++it) {
            int i = tid + 256 * it;
            int mat = i / 3456, rem = i % 3456, kr = rem / 216, cc = rem % 216;
            *(u32x4*)(T + (mat * 16 + kr) * TS + 8 * cc) = t[it];
        }
        __syncthreads();
#pragma unroll 9
        for (int it = 0; it < 27; ++it) {
            int i = tid + 256 * it;
            int uu = i >> 6, l = i & 63;
            int ii = l & 31, hh = l >> 5;
            int mat = ii >> 4, col = 16 * uu + (ii & 15);
            const u16* tp = T + (mat * 16 + 8 * hh) * TS + col;
            u32x4 o;
#pragma unroll
            for (int j = 0; j < 4; ++j) o[j] = (u32)tp[(2 * j) * TS] | ((u32)tp[(2 * j + 1) * TS] << 16);
            *(u32x4*)(W1p + ((long)(uu * KS1 + ks) * 64 + l) * 8) = o;
        }
    } else if (bid < NCONV + NREP1 + NREP2) {
        const int s0 = 2 * (bid - NCONV - NREP1);
        u16* T = (u16*)lds;
        constexpr int TS = 648;
        u32x4 t[20];
#pragma unroll
        for (int it = 0; it < 20; ++it) {
            int i = tid + 256 * it;
            int fr = i / 80, cc = i % 80;
            t[it] = *(const u32x4*)(wd + (long)(32 * s0 + fr) * D_ + 8 * cc);
        }
#pragma unroll
        for (int it = 0; it < 20; ++it) {
            int i = tid + 256 * it;
            int fr = i / 80, cc = i % 80;
            *(u32x4*)(T + fr * TS + 8 * cc) = t[it];
        }
        __syncthreads();
#pragma unroll 10
        for (int it = 0; it < 20; ++it) {
            int i = tid + 256 * it;
            int sl = i / 2560, rem = i % 2560;
            int nt = rem >> 6, l = rem & 63;
            int idx = l & 15, kq = l >> 4;
            int fb = 32 * sl + 16 * (kq >> 1) + 4 * (kq & 1);
            const u16* tp = T + 16 * nt + idx;
            u32x4 o;
            o[0] = (u32)tp[(fb + 0) * TS] | ((u32)tp[(fb + 1) * TS] << 16);
            o[1] = (u32)tp[(fb + 2) * TS] | ((u32)tp[(fb + 3) * TS] << 16);
            o[2] = (u32)tp[(fb + 8) * TS] | ((u32)tp[(fb + 9) * TS] << 16);
            o[3] = (u32)tp[(fb + 10) * TS] | ((u32)tp[(fb + 11) * TS] << 16);
            *(u32x4*)(W2p + ((long)(nt * KS2 + s0 + sl) * 64 + l) * 8) = o;
        }
    } else {
        const int cb = bid - NCONV - NREP1 - NREP2;
        const int cp = tid & 31, kg = tid >> 5;
        float a0 = 0.f, a1 = 0.f, b0 = 0.f, b1 = 0.f;
#pragma unroll 20
        for (int i = 0; i < 80; ++i) {
            u32 wv = *(const u32*)(wg + (long)(kg * 80 + i) * F_ + cb * 64 + 2 * cp);
            u32 uv = *(const u32*)(wu + (long)(kg * 80 + i) * F_ + cb * 64 + 2 * cp);
            a0 += lo2f(wv); a1 += hi2f(wv);
            b0 += lo2f(uv); b1 += hi2f(uv);
        }
        float* T = (float*)lds;
        T[kg * 64 + 2 * cp] = a0;
        T[kg * 64 + 2 * cp + 1] = a1;
        T[512 + kg * 64 + 2 * cp] = b0;
        T[512 + kg * 64 + 2 * cp + 1] = b1;
        __syncthreads();
        if (tid < 128) {
            int mat = tid >> 6, c = tid & 63;
            float t = 0.f;
#pragma unroll
            for (int g = 0; g < 8; ++g) t += T[mat * 512 + g * 64 + c];
            int f = cb * 64 + c;
            cs[(f >> 4) * 32 + mat * 16 + (f & 15)] = t;
        }
    }
}

constexpr int G1_STAGE = 44 * 1024;
constexpr int G1_CS_OFF = 3 * G1_STAGE;

typedef __attribute__((address_space(1))) unsigned long long gu64;
typedef __attribute__((address_space(1))) unsigned int gu32;
typedef __amdgpu_buffer_rsrc_t rsrc_t;
__device__ __forceinline__ rsrc_t mk_rsrc(const void* p, u32 bytes) {
    return __builtin_amdgcn_make_buffer_rsrc((void*)p, 0, bytes, 0x00020000);
}
__device__ __forceinline__ void st16_dev(rsrc_t r, u32 off, u32x4 v) { __builtin_amdgcn_raw_buffer_store_b128(v, r, off, 0, 16); }
__device__ __forceinline__ u32x4 ld16_dev(rsrc_t r, u32 off) { return __builtin_amdgcn_raw_buffer_load_b128(r, off, 0, 16); }

__device__ __forceinline__ void wait_count(u32* cnt, u32 target) {
    for (unsigned spins = 0; spins < (1u << 22); ++spins) {
        u32 v = __hip_atomic_load((gu32*)cnt, __ATOMIC_RELAXED, __HIP_MEMORY_SCOPE_AGENT);
        if (v >= target) break;
        __builtin_amdgcn_s_sleep(2);
    }
    __builtin_amdgcn_fence(__ATOMIC_ACQUIRE, "wavefront");
    asm volatile("" ::: "memory");
}
__device__ __forceinline__ void signal_count(u32* cnt) {
    asm volatile("s_waitcnt vmcnt(0)" ::: "memory");
    __syncthreads();
    if (threadIdx.x == 0) __hip_atomic_fetch_add((gu32*)cnt, 1u, __ATOMIC_RELAXED, __HIP_MEMORY_SCOPE_AGENT);
}

__device__ __forceinline__ void g1_epi_unit(const f32x16& a, const char* cp, float mb, float rinv, rsrc_t hr,
                                            int token, int u, int hh) {
    f32x4 c0 = *(const f32x4*)(cp), c1 = *(const f32x4*)(cp + 32);
    f32x4 c2 = *(const f32x4*)(cp + 64), c3 = *(const f32x4*)(cp + 96);
    float hv[8];
#pragma unroll
    for (int e = 0; e < 8; ++e) {
        float cg = (e < 4) ? c0[e & 3] : c1[e & 3];
        float cu = (e < 4) ? c2[e & 3] : c3[e & 3];
        u32 gu = pack2((a[e] - mb * cg) * rinv, (a[e + 8] - mb * cu) * rinv);
        float g = lo2f(gu), uu = hi2f(gu);
        float sg = rbf(__builtin_amdgcn_rcpf(1.0f + __expf(-g)));
        hv[e] = sg * uu;
    }
    u32x4 o = {pack2(hv[0], hv[1]), pack2(hv[2], hv[3]), pack2(hv[4], hv[5]), pack2(hv[6], hv[7])};
    u32 idx = (u32)(((token >> 4) * KS2 + (u >> 1)) * 64 + (token & 15) + 16 * (2 * (u & 1) + hh));
    st16_dev(hr, idx * 16, o);
}

constexpr int G1_ST_OFF = G1_CS_OFF + 1024;
constexpr int FFN_LDS = 3 * G1_STAGE + 1024 + 4096;
constexpr u32 HP_BYTES = 128u * KS2 * 1024u;
constexpr u32 X_BYTES = (u32)S_ * D_ * 2u;
__device__ __forceinline__ void g1_stats(const char* lds, int tb, int tk, float& mb, float& rinv) {
    f32x2 a = *(const f32x2*)(lds + G1_ST_OFF + (tb * 64 + tk) * 8);
    f32x2 b = *(const f32x2*)(lds + G1_ST_OFF + (tb * 64 + tk + 32) * 8);
    f32x2 c = *(const f32x2*)(lds + G1_ST_OFF + 2048 + (tb * 64 + tk) * 8);
    f32x2 d = *(const f32x2*)(lds + G1_ST_OFF + 2048 + (tb * 64 + tk + 32) * 8);
    float s1 = (a[0] + b[0]) + (c[0] + d[0]), s2 = (a[1] + b[1]) + (c[1] + d[1]);
    const float mean = s1 * (1.0f / 640.0f);
    float var = s2 * (1.0f / 640.0f) - mean * mean;
    var = var > 0.f ? var : 0.f;
    ln_finish(mean, var, mb, rinv);
}

template <bool WAIT>
__device__ __forceinline__ void g1_phase(char* lds, const u16* __restrict__ xF, const u16* __restrict__ W1p,
                                         const float* __restrict__ cs, u16* __restrict__ hp, u32* wcnt) {
    const int tid = threadIdx.x, lane = tid & 63;
    const int w = __builtin_amdgcn_readfirstlane(tid >> 6);
    const int xcd = blockIdx.x & 7, jj = blockIdx.x >> 3;
    const int rt = 8 * (xcd >> 2) + (jj & 7), ct = 4 * (xcd & 3) + (jj >> 3);
    const int ubase = ct * 7;
    const int tb = w & 3;
    const int tk = lane & 31, hh = lane >> 5;
    const int token = 128 * rt + 32 * tb + tk;
    const rsrc_t hr = mk_rsrc(hp, HP_BYTES);
    u32 lb[3];
#pragma unroll
    for (int b = 0; b < 3; ++b) { lb[b] = (u32)(b * G1_STAGE + lane * 16); asm volatile("" : "+v"(lb[b])); }

    if (w >= 4) {
        const int lw = w - 4;
        const char* wbase = (const char*)W1p;
        const char* xbase = (const char*)xF;
        u32 srcw[7];
        const u32 srcx = (u32)((((4 * rt + lw) * KS1) * 64 + lane) * 16);
#pragma unroll
        for (int i = 0; i < 7; ++i) {
            int q = lw * 7 + i;
            int uu = ubase + (q >> 2);
            if (uu > NUNIT - 1) uu = NUNIT - 1;
            srcw[i] = (u32)(((uu * KS1 + (q & 3)) * 64 + lane) * 16);
        }
#define G1_ISSUE_W(s) do { \
        _Pragma("unroll") for (int i = 0; i < 7; ++i) \
            __builtin_amdgcn_global_load_lds((const u32*)(wbase + (srcw[i] + (u32)((s) * 4096))), \
                (u32*)(lds + ((s) % 3) * G1_STAGE + (16 + lw * 7 + i) * 1024 + lane * 16), 16, 0, 0); \
        } while (0)
#define G1_ISSUE_X(s) do { \
        _Pragma("unroll") for (int i = 0; i < 4; ++i) \
            __builtin_amdgcn_global_load_lds((const u32*)(xbase + (srcx + (u32)((s) * 4096 + i * 1024))), \
                (u32*)(lds + ((s) % 3) * G1_STAGE + (lw * 4 + i) * 1024 + lane * 16), 16, 0, WAIT ? 16 : 0); \
        } while (0)
        G1_ISSUE_W(0);
        G1_ISSUE_W(1);
        if (WAIT) wait_count(wcnt, 16);
        G1_ISSUE_X(0);
        G1_ISSUE_X(1);
        float sa[4] = {0.f, 0.f, 0.f, 0.f}, sb[4] = {0.f, 0.f, 0.f, 0.f};
#pragma unroll
        for (int s = 0; s < 10; ++s) {
            if (s == 0) asm volatile("s_waitcnt vmcnt(4) lgkmcnt(0)" ::: "memory");
            else if (s < 9) asm volatile("s_waitcnt vmcnt(11) lgkmcnt(0)" ::: "memory");
            else asm volatile("s_waitcnt vmcnt(0) lgkmcnt(0)" ::: "memory");
            __builtin_amdgcn_s_barrier();
            asm volatile("" ::: "memory");
            u32x4 xx[2];
#pragma unroll
            for (int j = 0; j < 2; ++j) xx[j] = *(const u32x4*)(lds + lb[s % 3] + tb * 4096 + j * 1024);
            if (s + 2 < 10) { G1_ISSUE_W(s + 2); G1_ISSUE_X(s + 2); }
#pragma unroll
            for (int j = 0; j < 2; ++j)
#pragma unroll
                for (int e = 0; e < 4; ++e) {
                    float lo = lo2f(xx[j][e]), hi = hi2f(xx[j][e]);
                    sa[e] += lo + hi;
                    sb[e] = __builtin_fmaf(lo, lo, __builtin_fmaf(hi, hi, sb[e]));
                }
        }
#undef G1_ISSUE_W
#undef G1_ISSUE_X
        float s1 = (sa[0] + sa[1]) + (sa[2] + sa[3]), s2 = (sb[0] + sb[1]) + (sb[2] + sb[3]);
        { f32x2 st = {s1, s2}; *(f32x2*)(lds + G1_ST_OFF + (tb * 64 + lane) * 8) = st; }
        __syncthreads();
        float mb, rinv;
        g1_stats(lds, tb, tk, mb, rinv);
#pragma unroll
        for (int i = 4; i < 7; ++i) {
            int u = ubase + i;
            if (u < NUNIT) {
                f32x16 a;
#pragma unroll
                for (int r = 0; r < 4; ++r) {
                    f32x4 t = *(const f32x4*)(lds + G1_STAGE + (tb * 12 + (i - 4) * 4 + r) * 1024 + lane * 16);
                    a[4 * r] = t[0]; a[4 * r + 1] = t[1]; a[4 * r + 2] = t[2]; a[4 * r + 3] = t[3];
                }
                g1_epi_unit(a, lds + G1_CS_OFF + i * 128 + hh * 16, mb, rinv, hr, token, u, hh);
            }
        }
    } else {
        if (lane < 56) {
            int e = ubase * 32 + w * 56 + lane;
            float csv = (e < NUNIT * 32) ? cs[e] : 0.f;
            *(float*)(lds + G1_CS_OFF + (w * 56 + lane) * 4) = csv;
        }
        f32x16 acc[7];
#pragma unroll
        for (int a = 0; a < 7; ++a)
#pragma unroll
            for (int i = 0; i < 16; ++i) acc[a][i] = 0.f;
        bf16x8 xf[3], wf[3][7];
        float sa[2] = {0.f, 0.f}, sb[2] = {0.f, 0.f};
#pragma unroll
        for (int g = 0; g < 42; ++g) {
            const int s = g >> 2, j = g & 3;
            if (g < 40) {
                if (j == 0) {
                    asm volatile("s_waitcnt lgkmcnt(0)" ::: "memory");
                    __builtin_amdgcn_s_barrier();
                    asm volatile("" ::: "memory");
                }
                xf[g % 3] = *(const bf16x8*)(lds + lb[s % 3] + tb * 4096 + j * 1024);
#pragma unroll
                for (int i = 0; i < 7; ++i) wf[g % 3][i] = *(const bf16x8*)(lds + lb[s % 3] + (4 + i) * 4096 + j * 1024);
            }
            if (g >= 2) {
                const int c = (g - 2) % 3;
#pragma unroll
                for (int i = 0; i < 7; ++i)
                    acc[i] = __builtin_amdgcn_mfma_f32_32x32x16_bf16(wf[c][i], xf[c], acc[i], 0, 0, 0);
                if (((g - 2) & 3) >= 2) {
                    u32x4 xx = __builtin_bit_cast(u32x4, xf[c]);
#pragma unroll
                    for (int e = 0; e < 4; ++e) {
                        float lo = lo2f(xx[e]), hi = hi2f(xx[e]);
                        sa[e & 1] += lo + hi;
                        sb[e & 1] = __builtin_fmaf(lo, lo, __builtin_fmaf(hi, hi, sb[e & 1]));
                    }
                }
#pragma unroll
                for (int i = 0; i < 7; ++i) {
                    __builtin_amdgcn_sched_group_barrier(0x8, 1, 0);
                    __builtin_amdgcn_sched_group_barrier(0x100, 1, 0);
                    __builtin_amdgcn_sched_group_barrier(0x2, 4, 0);
                }
            }
            __builtin_amdgcn_sched_barrier(0);
        }
#pragma unroll
        for (int i = 4; i < 7; ++i)
#pragma unroll
            for (int r = 0; r < 4; ++r) {
                f32x4 t = {acc[i][4 * r], acc[i][4 * r + 1], acc[i][4 * r + 2], acc[i][4 * r + 3]};
                *(f32x4*)(lds + G1_STAGE + (tb * 12 + (i - 4) * 4 + r) * 1024 + lane * 16) = t;
            }
        { f32x2 st = {sa[0] + sa[1], sb[0] + sb[1]}; *(f32x2*)(lds + G1_ST_OFF + 2048 + (tb * 64 + lane) * 8) = st; }
        __syncthreads();
        float mb, rinv;
        g1_stats(lds, tb, tk, mb, rinv);
#pragma unroll
        for (int i = 0; i < 4; ++i) {
            int u = ubase + i;
            if (u < NUNIT) g1_epi_unit(acc[i], lds + G1_CS_OFF + i * 128 + hh * 16, mb, rinv, hr, token, u, hh);
        }
    }
}

constexpr int PROW = 336;
constexpr int PTILE = 64 * PROW;
constexpr int PD2 = 3;

template <bool MID>
__device__ __forceinline__ void g2_phase(char* lds, const u16* __restrict__ hp, const u16* __restrict__ W2p,
                                         const u16* __restrict__ xresF, const u16* __restrict__ yF,
                                         u16* __restrict__ xout, unsigned long long* gran, u32* wcnt) {
    const int tid = threadIdx.x, lane = tid & 63;
    const int w = __builtin_amdgcn_readfirstlane(tid >> 6);
    const int xcd = blockIdx.x & 7, jj = blockIdx.x >> 3;
    const int rt = 8 * (xcd >> 1) + (jj & 7), ct = 4 * (xcd & 1) + (jj >> 3);

    const rsrc_t hr = mk_rsrc(hp, HP_BYTES);
    const u32 hoff = (u32)(((4 * rt) * KS2 * 64 + lane) * 16);
    const bf16x8* wbp = (const bf16x8*)W2p + (long)(5 * ct) * KS2 * 64 + lane;
    const int nsteps = (w < 6) ? 7 : 6;
#define LDH(m, s) __builtin_bit_cast(bf16x8, ld16_dev(hr, hoff + (u32)(((m) * KS2 + (s)) * 1024)))

    bf16x8 hf[PD2][4], wf[PD2][5];
#pragma unroll
    for (int i = 0; i < PD2; ++i) {
        int s = w + 8 * i;
#pragma unroll
        for (int n = 0; n < 5; ++n) wf[i][n] = wbp[(long)(n * KS2 + s) * 64];
    }
    const int tok = lane;
    const int token = 64 * rt + tok;
    const rsrc_t xr = mk_rsrc(xresF, X_BYTES);
    u32x4 res[2], yv[2];
#pragma unroll
    for (int it = 0; it < 2; ++it) {
        int cc = w + 8 * it;
        if (cc < 10) {
            long idx = xf_idx(token, 80 * ct + 8 * cc);
            res[it] = ld16_dev(xr, (u32)idx * 2);
            if (MID) yv[it] = *(const u32x4*)(yF + idx);
        }
    }
    wait_count(wcnt, 16);
#pragma unroll
    for (int i = 0; i < PD2; ++i) {
        int s = w + 8 * i;
#pragma unroll
        for (int m = 0; m < 4; ++m) hf[i][m] = LDH(m, s);
    }
    f32x4 acc[5][4];
#pragma unroll
    for (int n = 0; n < 5; ++n)
#pragma unroll
        for (int m = 0; m < 4; ++m) acc[n][m] = (f32x4){0.f, 0.f, 0.f, 0.f};
#pragma unroll
    for (int i = 0; i < 7; ++i) {
        if (i < nsteps) {
#pragma unroll
            for (int n = 0; n < 5; ++n)
#pragma unroll
                for (int m = 0; m < 4; ++m)
                    acc[n][m] = __builtin_amdgcn_mfma_f32_16x16x32_bf16(wf[i % PD2][n], hf[i % PD2][m], acc[n][m], 0, 0, 0);
            if (i + PD2 < nsteps) {
                int s = w + 8 * (i + PD2);
#pragma unroll
                for (int m = 0; m < 4; ++m) hf[i % PD2][m] = LDH(m, s);
#pragma unroll
                for (int n = 0; n < 5; ++n) wf[i % PD2][n] = wbp[(long)(n * KS2 + s) * 64];
            }
        }
    }
#undef LDH
    const int tl = lane & 15, q = lane >> 4;
    char* slot = lds + (w & 3) * PTILE + tl * PROW + q * 16;
    if (w >= 4) {
#pragma unroll
        for (int n = 0; n < 5; ++n)
#pragma unroll
            for (int m = 0; m < 4; ++m) *(f32x4*)(slot + m * 16 * PROW + n * 64) = acc[n][m];
    }
    __syncthreads();
    if (w < 4) {
#pragma unroll
        for (int n = 0; n < 5; ++n)
#pragma unroll
            for (int m = 0; m < 4; ++m) {
                f32x4 o = *(const f32x4*)(slot + m * 16 * PROW + n * 64);
                *(f32x4*)(slot + m * 16 * PROW + n * 64) = o + acc[n][m];
            }
    }
    __syncthreads();
    u32x4 x2[2];
#pragma unroll
    for (int it = 0; it < 2; ++it) {
        int cc = w + 8 * it;
        if (cc < 10) {
            const char* p = lds + tok * PROW + cc * 32;
            f32x4 s0 = *(const f32x4*)(p), s1 = *(const f32x4*)(p + 16);
#pragma unroll
            for (int k = 1; k < 4; ++k) {
                s0 += *(const f32x4*)(p + k * PTILE);
                s1 += *(const f32x4*)(p + k * PTILE + 16);
            }
            u32x4 rs = res[it];
            x2[it][0] = pack2(lo2f(rs[0]) + rbf(s0[0]), hi2f(rs[0]) + rbf(s0[1]));
            x2[it][1] = pack2(lo2f(rs[1]) + rbf(s0[2]), hi2f(rs[1]) + rbf(s0[3]));
            x2[it][2] = pack2(lo2f(rs[2]) + rbf(s1[0]), hi2f(rs[2]) + rbf(s1[1]));
            x2[it][3] = pack2(lo2f(rs[3]) + rbf(s1[2]), hi2f(rs[3]) + rbf(s1[3]));
            if (!MID) *(u32x4*)(xout + (long)token * D_ + 80 * ct + 8 * cc) = x2[it];
        }
    }
    if (MID) {
        __syncthreads();
        float* red = (float*)lds;
        float* red2 = (float*)(lds + 8192);
#pragma unroll
        for (int it = 0; it < 2; ++it) {
            int cc = w + 8 * it;
            if (cc < 10) {
                float a = 0.f, b = 0.f;
#pragma unroll
                for (int e = 0; e < 4; ++e) {
                    float lo = lo2f(x2[it][e]), hi = hi2f(x2[it][e]);
                    a += lo + hi;
                    b = __builtin_fmaf(lo, lo, __builtin_fmaf(hi, hi, b));
                }
                red[cc * 64 + tok] = a;
                red[640 + cc * 64 + tok] = b;
            }
        }
        __syncthreads();
        if (w < 2) {
            float t = 0.f;
#pragma unroll
            for (int c = 0; c < 10; ++c) t += red[w * 640 + c * 64 + tok];
            unsigned long long g = ((unsigned long long)GTAG << 32) | (unsigned long long)__float_as_uint(t);
            __hip_atomic_store((gu64*)(gran + ((rt * 8 + ct) * 2 + w) * 64 + tok), g, __ATOMIC_RELAXED, __HIP_MEMORY_SCOPE_AGENT);
        }
        {
            unsigned long long g0, g1;
            gu64* p0 = (gu64*)(gran + ((rt * 8 + w) * 2 + 0) * 64 + tok);
            gu64* p1 = (gu64*)(gran + ((rt * 8 + w) * 2 + 1) * 64 + tok);
            for (unsigned spins = 0; spins < (1u << 22); ++spins) {
                g0 = __hip_atomic_load(p0, __ATOMIC_RELAXED, __HIP_MEMORY_SCOPE_AGENT);
                g1 = __hip_atomic_load(p1, __ATOMIC_RELAXED, __HIP_MEMORY_SCOPE_AGENT);
                bool ok = ((u32)(g0 >> 32) == GTAG) && ((u32)(g1 >> 32) == GTAG);
                if (__all(ok)) break;
                __builtin_amdgcn_s_sleep(1);
            }
            red2[w * 64 + tok] = __uint_as_float((u32)g0);
            red2[512 + w * 64 + tok] = __uint_as_float((u32)g1);
        }
        __syncthreads();
        float a = 0.f, b = 0.f;
#pragma unroll
        for (int c = 0; c < 8; ++c) { a += red2[c * 64 + tok]; b += red2[512 + c * 64 + tok]; }
        const float mean = a * (1.0f / 640.0f);
        float var = b * (1.0f / 640.0f) - mean * mean;
        var = var > 0.f ? var : 0.f;
        float mb, rinv;
        ln_finish(mean, var, mb, rinv);
        const rsrc_t xo = mk_rsrc(xout, X_BYTES);
#pragma unroll
        for (int it = 0; it < 2; ++it) {
            int cc = w + 8 * it;
            if (cc < 10) {
                u32x4 o;
#pragma unroll
                for (int e = 0; e < 4; ++e) {
                    float xa = lo2f(x2[it][e]), xb = hi2f(x2[it][e]);
                    float qa = rbf(rbf(xa - mb) * rinv), qb = rbf(rbf(xb - mb) * rinv);
                    float ga = rbf(qa * lo2f(yv[it][e])), gb = rbf(qb * hi2f(yv[it][e]));
                    o[e] = pack2(xa + ga, xb + gb);
                }
                st16_dev(xo, (u32)xf_idx(token, 80 * ct + 8 * cc) * 2, o);
            }
        }
    }
}

__global__ __launch_bounds__(512, 2) void ffn_kernel(const u16* __restrict__ xaF, const u16* __restrict__ W1p,
                                                     const float* __restrict__ cs, u16* __restrict__ hpA,
                                                     u16* __restrict__ hpB, const u16* __restrict__ W2p,
                                                     const u16* __restrict__ yF, u16* __restrict__ xbF,
                                                     u16* __restrict__ out, unsigned long long* gran, u32* cnt) {
    __shared__ __attribute__((aligned(16))) char lds[FFN_LDS];
    const int xcd = blockIdx.x & 7, jj = blockIdx.x >> 3;
    const int rt1 = 8 * (xcd >> 2) + (jj & 7);
    const int rg2 = (8 * (xcd >> 1) + (jj & 7)) >> 1;
    g1_phase<false>(lds, xaF, W1p, cs, hpA, nullptr);
    signal_count(cnt + (0 * 16 + rt1) * 32);
    g2_phase<true>(lds, hpA, W2p, xaF, yF, xbF, gran, cnt + (0 * 16 + rg2) * 32);
    signal_count(cnt + (1 * 16 + rg2) * 32);
    g1_phase<true>(lds, xbF, W1p, cs, hpB, cnt + (1 * 16 + rt1) * 32);
    signal_count(cnt + (2 * 16 + rt1) * 32);
    g2_phase<false>(lds, hpB, W2p, xbF, yF, out, gran, cnt + (2 * 16 + rg2) * 32);
}

extern "C" void kernel_launch(void* const* d_in, const int* in_sizes, int n_in,
                              void* d_out, int out_size, void* d_ws, size_t ws_size,
                              hipStream_t stream) {
    const u16* q = (const u16*)d_in[0];
    const u16* k = (const u16*)d_in[1];
    const u16* v = (const u16*)d_in[2];
    const u16* sk = (const u16*)d_in[3];
    const u16* ssmk = (const u16*)d_in[4];
    const u16* ssmD = (const u16*)d_in[5];
    const u16* wg = (const u16*)d_in[7];
    const u16* wu = (const u16*)d_in[8];
    const u16* wd = (const u16*)d_in[9];
    u16* out = (u16*)d_out;

    char* ws = (char*)d_ws;
    constexpr size_t SZ_X = (size_t)S_ * D_ * 2;
    size_t off = 0;
    unsigned long long* gran = (unsigned long long*)(ws + off); off += (size_t)GRAN_WORDS * 8;
    u32* cnt = (u32*)(ws + off); off += 8192;
    u16* W1p = (u16*)(ws + off); off += (size_t)NUNIT * KS1 * 1024;
    u16* kvR = (u16*)(ws + off); off += SZ_X;
    u16* yF = (u16*)(ws + off); off += SZ_X;
    u16* xaF = (u16*)(ws + off); off += SZ_X;
    u16* xbF = (u16*)(ws + off); off += SZ_X;
    u16* qF = (u16*)(ws + off); off += SZ_X;
    u16* W2p = (u16*)(ws + off); off += (size_t)40 * KS2 * 1024;
    u16* hpA = (u16*)(ws + off); off += (size_t)128 * KS2 * 1024;
    u16* hpB = (u16*)(ws + off); off += (size_t)128 * KS2 * 1024;
    f32x2* qs = (f32x2*)(ws + off); off += (size_t)S_ * 8;
    float* cs = (float*)(ws + off); off += (size_t)NUNIT * 32 * 4;

    prep_kernel<<<320 + 512, 256, 0, stream>>>(k, v, sk, kvR, q, qF, qs, (u32x4*)gran);
    conv_kernel<<<NCONV + NREP1 + NREP2 + NCS, 256, 0, stream>>>(kvR, ssmk, ssmD, yF, qF, qs, xaF, wg, wu, wd, W1p, W2p, cs);
    ffn_kernel<<<256, 512, 0, stream>>>(xaF, W1p, cs, hpA, hpB, W2p, yF, xbF, out, gran, cnt);
}
```
